```python
import torch

def _softmax(x, axis=-1):
    return torch.softmax(x, dim=axis)
def _gelu(x):
    return torch.nn.functional.gelu(x, approximate="tanh")
def _silu(x):
    return torch.nn.functional.silu(x)
def _sigmoid(x):
    return torch.sigmoid(x)
def _layernorm(x, axis=-1, eps=1e-5):
    m = x.mean(dim=axis, keepdim=True)
    v = x.var(dim=axis, keepdim=True, unbiased=False)
    return (x - m) / torch.sqrt(v + eps)
def _scatter_add(tgt, idx, src, axis=0):
    return tgt.clone().index_add_(axis, idx, src)
def _segment_sum(src, seg, n):
    out = torch.zeros((n,) + src.shape[1:], dtype=src.dtype)
    return out.index_add_(0, seg, src)
def _pad(x, pw):
    flat = tuple(v for pair in reversed(pw) for v in pair)
    return torch.nn.functional.pad(x, flat)

kernel_name = 'concat_heads__sliding_block_cached__compressed_kv_cla__decode'

def setup_inputs():

    _w = (lambda a: (lambda t: t.to(torch.bfloat16) if t.is_floating_point() else t)(torch.as_tensor(a)))
    def _A_setup():
        B, Sq, D = 4, 1, 512
        Hq, Hkv, Sk, Dh = 32, 32, 4096, 128
        assert Hq % Hkv == 0, (Hq, Hkv)
        return {
            "x": _w((lambda *s: torch.randn(*s, dtype=torch.bfloat16) * 0.8)(B, Sq, D)),
            "k_cache": _w((lambda *s: torch.randn(*s, dtype=torch.bfloat16) * 0.8)(B, Hkv, Sk, Dh)),
            "v_cache": _w((lambda *s: torch.randn(*s, dtype=torch.bfloat16) * 0.05)(B, Hkv, Sk, Dh)),
            "wq": _w((lambda *s: torch.randn(*s, dtype=torch.bfloat16) * 0.8)(D, Hq * Dh) * (D ** -0.5)),
            "wo": _w((lambda *s: torch.randn(*s, dtype=torch.bfloat16) * 0.05)(Hq * Dh, D) * ((Hq * Dh) ** -0.5)),
        }
    _A_in = _A_setup()

    def _B_setup():
        B, H, Sq, Sk, D, M = 4, 32, 1, 4096, 128, 512
        Dc = 4 * D
        return {
            "x": _w((lambda *s: torch.randn(*s, dtype=torch.bfloat16) * 1.6)(B, Sq, M)),
            "c_kv": _w((lambda *s: torch.randn(*s, dtype=torch.bfloat16) * 0.1)(B, Sk, Dc)),
            "w_q": _w((lambda *s: torch.randn(*s, dtype=torch.bfloat16) * 3.2)(M, H, D) * (M ** -0.5)),
            "w_uk": _w((lambda *s: torch.randn(*s, dtype=torch.bfloat16) * 3.2)(Dc, H, D) * (Dc ** -0.5)),
            "w_uv": _w((lambda *s: torch.randn(*s, dtype=torch.bfloat16) * 0.1)(Dc, H, D) * (Dc ** -0.5)),
            "w_o": _w((lambda *s: torch.randn(*s, dtype=torch.bfloat16) * 0.1)(H, D, M) * ((H * D) ** -0.5)),
        }
    _B_in = _B_setup()

    return {
        "A_x": _A_in["x"],
        "A_k_cache": _A_in["k_cache"],
        "A_v_cache": _A_in["v_cache"],
        "A_wq": _A_in["wq"],
        "A_wo": _A_in["wo"],
        "B_x": _B_in["x"],
        "B_c_kv": _B_in["c_kv"],
        "B_w_q": _B_in["w_q"],
        "B_w_uk": _B_in["w_uk"],
        "B_w_uv": _B_in["w_uv"],
        "B_w_o": _B_in["w_o"],
    }

def reference(A_x, A_k_cache, A_v_cache, A_wq, A_wo, B_x, B_c_kv, B_w_q, B_w_uk, B_w_uv, B_w_o):
    def _A_ref(x, k_cache, v_cache, wq, wo):
        B, Sq, D = x.shape
        Hq, Dh = 32, 128
        Hkv, Sk = k_cache.shape[1], k_cache.shape[2]
        W = min(4096, max(256, Sk // 4))
        rep = Hq // Hkv
        q = (lambda x, *d: x.permute(*d))((x @ wq).reshape(B, Sq, Hq, Dh), 0, 2, 1, 3)
        k = (lambda x, n, axis: torch.repeat_interleave(x, n, dim=axis))(k_cache[:, :, Sk - W:, :], rep, axis=1)
        v = (lambda x, n, axis: torch.repeat_interleave(x, n, dim=axis))(v_cache[:, :, Sk - W:, :], rep, axis=1)
        scores = torch.einsum("bhqd,bhkd->bhqk", q, k) * (Dh ** -0.5)
        p = _softmax(scores, axis=-1)
        o = torch.einsum("bhqk,bhkd->bhqd", p, v)
        return (lambda x, *d: x.permute(*d))(o, 0, 2, 1, 3).reshape(B, Sq, Hq * Dh) @ wo

    def _B_ref(x, c_kv, w_q, w_uk, w_uv, w_o):
        B, Sq, M = x.shape[0], x.shape[1], x.shape[2]
        Sk = c_kv.shape[1]
        H, D = w_q.shape[1], w_q.shape[2]
        q = torch.einsum("bqm,mhd->bhqd", x, w_q)
        k = torch.einsum("bkc,chd->bhkd", c_kv, w_uk)
        v = torch.einsum("bkc,chd->bhkd", c_kv, w_uv)
        scores = torch.einsum("bhqd,bhkd->bhqk", q, k) * (D ** -0.5)
        off = Sk - Sq
        qi = torch.arange(Sq)[:, None]
        kj = torch.arange(Sk)[None, :]
        cmask = kj <= (qi + off)
        scores = torch.where(cmask[None, None], scores, -1e9)
        p = _softmax(scores, axis=-1)
        ctx = torch.einsum("bhqk,bhkd->bhqd", p, v)
        return torch.einsum("bhqd,hdm->bqm", ctx, w_o)

    a = _A_ref(A_x, A_k_cache, A_v_cache, A_wq, A_wo)
    b = _B_ref(B_x, B_c_kv, B_w_q, B_w_uk, B_w_uv, B_w_o)
    return torch.concatenate([a, b], axis=-1)
```

```cpp
#define PROLE 0
#define PIDX 0
#pragma clang diagnostic ignored "-Wint-to-pointer-cast"
#include <hip/hip_runtime.h>
#include <hip/hip_bf16.h>
#include <stdint.h>

typedef unsigned int u32;
typedef unsigned short u16;
typedef unsigned long long u64;
using u32x4  = __attribute__((ext_vector_type(4))) unsigned;
using u32x2  = __attribute__((ext_vector_type(2))) unsigned;
using f32x4  = __attribute__((ext_vector_type(4))) float;
using f32x16 = __attribute__((ext_vector_type(16))) float;
using bf16x8 = __attribute__((ext_vector_type(8))) short;
using s16x4  = __attribute__((ext_vector_type(4))) short;
#define DI __device__ __forceinline__
#define LDSP __attribute__((address_space(3)))
#define SCHED_FENCE() __builtin_amdgcn_sched_barrier(0)
#define PIN(x) asm volatile("" : "+v"(x))

DI float bflo(u32 v) { return __uint_as_float(v << 16); }
DI float bfhi(u32 v) { return __uint_as_float(v & 0xffff0000u); }
using f32x2_ = __attribute__((ext_vector_type(2))) float;
using bf16x2_ = __attribute__((ext_vector_type(2))) __bf16;
DI u32 pk2(float lo, float hi) { return __builtin_bit_cast(u32, __builtin_convertvector(f32x2_{lo, hi}, bf16x2_)); }
template <int CTRL> DI float dppf(float v) { return __builtin_bit_cast(float, __builtin_amdgcn_update_dpp(0, __builtin_bit_cast(int, v), CTRL, 0xf, 0xf, true)); }
template <int CTRL> DI u32 dppu(u32 v) { return (u32)__builtin_amdgcn_update_dpp(0, (int)v, CTRL, 0xf, 0xf, true); }
DI float row16_sum(float v) { v += dppf<0x128>(v); v += dppf<0x124>(v); v += dppf<0x122>(v); v += dppf<0x121>(v); return v; }
DI float row16_max(float v) { v = fmaxf(v, dppf<0x128>(v)); v = fmaxf(v, dppf<0x124>(v)); v = fmaxf(v, dppf<0x122>(v)); v = fmaxf(v, dppf<0x121>(v)); return v; }
DI float row16_maxq(float v) {
  asm volatile("s_nop 1\n\tv_max_f32_dpp %0, %0, %0 row_ror:8 row_mask:0xf bank_mask:0xf\n\ts_nop 1\n\tv_max_f32_dpp %0, %0, %0 row_ror:4 row_mask:0xf bank_mask:0xf\n\t"
               "s_nop 1\n\tv_max_f32_dpp %0, %0, %0 row_ror:2 row_mask:0xf bank_mask:0xf\n\ts_nop 1\n\tv_max_f32_dpp %0, %0, %0 row_ror:1 row_mask:0xf bank_mask:0xf\n\ts_nop 1" : "+v"(v));
  return v;
}
DI float row16_sumq(float v) {
  asm volatile("s_nop 1\n\tv_add_f32_dpp %0, %0, %0 row_ror:8 row_mask:0xf bank_mask:0xf\n\ts_nop 1\n\tv_add_f32_dpp %0, %0, %0 row_ror:4 row_mask:0xf bank_mask:0xf\n\t"
               "s_nop 1\n\tv_add_f32_dpp %0, %0, %0 row_ror:2 row_mask:0xf bank_mask:0xf\n\ts_nop 1\n\tv_add_f32_dpp %0, %0, %0 row_ror:1 row_mask:0xf bank_mask:0xf\n\ts_nop 1" : "+v"(v));
  return v;
}
DI float x16_sum(float v) { auto s = __builtin_amdgcn_permlane16_swap(__float_as_uint(v), __float_as_uint(v), false, false); return __uint_as_float(s[0]) + __uint_as_float(s[1]); }
DI float x32_sum(float v) { auto s = __builtin_amdgcn_permlane32_swap(__float_as_uint(v), __float_as_uint(v), false, false); return __uint_as_float(s[0]) + __uint_as_float(s[1]); }
DI float x16_max(float v) { auto s = __builtin_amdgcn_permlane16_swap(__float_as_uint(v), __float_as_uint(v), false, false); return fmaxf(__uint_as_float(s[0]), __uint_as_float(s[1])); }
DI float x32_max(float v) { auto s = __builtin_amdgcn_permlane32_swap(__float_as_uint(v), __float_as_uint(v), false, false); return fmaxf(__uint_as_float(s[0]), __uint_as_float(s[1])); }
using bf16x2 = __attribute__((ext_vector_type(2))) __bf16;
DI float dot2(u32 a, u32 b, float c) { return __builtin_amdgcn_fdot2_f32_bf16(__builtin_bit_cast(bf16x2, a), __builtin_bit_cast(bf16x2, b), c, false); }
DI u32 plo(u32 r1, u32 r0) { return __builtin_amdgcn_perm(r1, r0, 0x05040100u); }
DI u32 phi(u32 r1, u32 r0) { return __builtin_amdgcn_perm(r1, r0, 0x07060302u); }
DI bf16x8 bfrag(const u32x4* R, int j) {
  const int q = j >> 1;
  u32x4 o;
  if (j & 1) { o[0] = phi(R[1][q], R[0][q]); o[1] = phi(R[3][q], R[2][q]); o[2] = phi(R[5][q], R[4][q]); o[3] = phi(R[7][q], R[6][q]); }
  else       { o[0] = plo(R[1][q], R[0][q]); o[1] = plo(R[3][q], R[2][q]); o[2] = plo(R[5][q], R[4][q]); o[3] = plo(R[7][q], R[6][q]); }
  return __builtin_bit_cast(bf16x8, o);
}
DI s16x4 kfrag(const u32x4* R, int j) {
  const int q = j >> 1;
  u32x2 o;
  if (j & 1) { o[0] = phi(R[1][q], R[0][q]); o[1] = phi(R[3][q], R[2][q]); }
  else       { o[0] = plo(R[1][q], R[0][q]); o[1] = plo(R[3][q], R[2][q]); }
  return __builtin_bit_cast(s16x4, o);
}
DI u32 selm(u32 m, u32 x, u32 y) { return __builtin_amdgcn_bitop3_b32(m, x, y, 0xca); }
DI float self(u32 m, float x, float y) { return __uint_as_float(selm(m, __float_as_uint(x), __float_as_uint(y))); }
DI f32x4 sel4(f32x4 a, f32x4 b, f32x4 c, f32x4 d, int r) { f32x4 lo = (r & 1) ? b : a, hi = (r & 1) ? d : c; return (r & 2) ? hi : lo; }
DI f32x4 mfma16(bf16x8 a, bf16x8 b, f32x4 c) { return __builtin_amdgcn_mfma_f32_16x16x32_bf16(a, b, c, 0, 0, 0); }
DI bf16x8 asbf(u32x4 v) { return __builtin_bit_cast(bf16x8, v); }
DI float dot8(const u32x4 kv, const u32x4 q) {
  float s = dot2(kv[0], q[0], 0.f);
  s = dot2(kv[1], q[1], s); s = dot2(kv[2], q[2], s); s = dot2(kv[3], q[3], s);
  return s;
}
constexpr int NB = 4, NH = 32, DH = 128, SK = 4096, WIN = 1024, DM = 512, DC = 512;
constexpr float LOG2E = 1.4426950408889634f;
constexpr float SCL2 = 0.088388347648318440f * LOG2E;

#define WOFF 8
#define AW 3
#define ASYNC 2
#define A0 4
#define PX 11
#define PY 16
#define CU 2
#define ND 3
#define NA0 4
#define NA1 26
#define TA0 500
#define TA1 235
#define WSA 2
#define NB0 3
#define WSB 2
#define N3 20
#define SP0 2
#define FLI 10
#define FLW 0
#define CPF 3
#define CPN 1
#define CPL 178
#define CPL2 40
#define FW2 3
#define WL1 15
#define WL2 16
#define THC 275

constexpr int C0 = 16 - 2 * ND;
constexpr int NCH = 16;
constexpr u32 WS_EPOCH = 0;
constexpr u32 WS_QP    = 4096;
constexpr u32 PBLK     = 4608;
constexpr u32 WS_PART  = WS_QP + NB * 8 * 384 * 16;
constexpr u32 WS_QX    = WS_PART + NB * 16 * 8 * PBLK;
constexpr u32 WS_ACC   = WS_QX + NH * NB * 4 * DH * 4;
constexpr u32 WS_ACX   = WS_ACC + 2 * 65536;
constexpr u32 WS_FLG   = WS_ACX + 32 * 4096;
constexpr u32 WS_XA    = WS_FLG + 128 * 128;
constexpr u32 WS_XB    = WS_XA + 64 * 2048 * 16;
constexpr u32 WS_XC    = WS_XB + 32 * 2048 * 16;
constexpr u32 WS_END   = WS_XC + 32 * 2048 * 16;
constexpr u32 WS_TS    = WS_END;
constexpr u32 RSRC_W3  = 0x00020000;
constexpr u32 SPIN_CAP = 1u << 17;

constexpr int L_T    = 0;
constexpr int L_Q    = 131072;
constexpr int L_P    = L_Q + 16384;
constexpr int L_MX   = L_P + 8192;
constexpr int L_SM   = L_MX + 512;
constexpr int L_S1P  = 12288;
constexpr int L_UKT  = 8192;
constexpr int L_QB   = L_Q;
constexpr int L_QO   = L_Q + 1024;
constexpr int L_LAT  = L_P;
constexpr int L_EX   = L_Q;
constexpr int L_ENDB = 163840;
constexpr int L_AP   = 0;
constexpr int L_AQ   = L_AP + 4096;
constexpr int L_AST  = L_AQ + 2048;
constexpr int L_AR2  = L_AST + 4352;
constexpr int L_AX   = L_AR2 + 16384;
constexpr int L_END  = L_ENDB;

DI u32 fsw(u32 key) { return (((key >> 1) & 1) << 2) | (((key >> 3) & 1) << 1); }
struct KArgs {
  const u16* xa; const u16* kc; const u16* vc; const u16* wqa; const u16* woa;
  const u16* xb; const u16* ckv; const u16* wqb; const u16* wuk; const u16* wuv; const u16* wob;
  char* ws; u16* out;
};

DI u32x4 ldnt(const u16* p) { return __builtin_nontemporal_load(reinterpret_cast<const u32x4*>(p)); }
DI u32x2 ldnt8(const u16* p) { return __builtin_nontemporal_load(reinterpret_cast<const u32x2*>(p)); }
DI u32x4 ldsh(const u16* p) { return *reinterpret_cast<const u32x4*>(p); }
DI u32x4 ld_sc1(__amdgpu_buffer_rsrc_t rs, u32 off) { return __builtin_amdgcn_raw_buffer_load_b128(rs, off, 0, 16); }
DI u32x2 ld_sc1_64(__amdgpu_buffer_rsrc_t rs, u32 off) { return __builtin_amdgcn_raw_buffer_load_b64(rs, off, 0, 16); }
DI void st_sc1(__amdgpu_buffer_rsrc_t rs, u32 off, u32x4 v) { __builtin_amdgcn_raw_buffer_store_b128(v, rs, off, 0, 16); }
DI void st_sc1_64(__amdgpu_buffer_rsrc_t rs, u32 off, u32x2 v) { __builtin_amdgcn_raw_buffer_store_b64(v, rs, off, 0, 16); }
DI void st_sc1_32(__amdgpu_buffer_rsrc_t rs, u32 off, u32 v) { __builtin_amdgcn_raw_buffer_store_b32(v, rs, off, 0, 16); }
DI void wg_barrier() { asm volatile("s_waitcnt lgkmcnt(0)\n\ts_barrier" ::: "memory"); }

#define TS(i) do { if (PROLE == ROLE_ && (i) == PIDX) tsx_ = __builtin_amdgcn_s_memrealtime(); } while (0)
#define TSARG , u64& tsx_, const int ROLE_
#define TSPASS , tsx_, ROLE_
#define TS_DECL u64 tsx_ = 0
#define TS_FLUSH() do { const u64 d_ = tsx_ ? tsx_ - ts0_ : 0; while (__builtin_amdgcn_s_memrealtime() - ts0_ < 4000 + 4 * d_) __builtin_amdgcn_s_sleep(2); } while (0)
template <int NARR, int XSTR>
DI float fx_val(__amdgpu_buffer_rsrc_t rs, u64 t, u32 x0, u32 tag) {
  float s = (float)__builtin_fma(__builtin_bit_cast(double, (t & 0x7ffffffffffffull) | 0x4330000000000000ull), 0x1p-40, -0x1.4p12);
  const u32 np = (u32)(t >> 51) & 127u;
  if (__builtin_expect(np != 0u, 0)) {
    float e = 0.f;
#pragma unroll 1
    for (u32 spins = 0; spins < 64; ++spins) {
      u32 c = 0; e = 0.f;
#pragma unroll 4
      for (u32 p = 0; p < NARR; ++p) {
        const u32x4 v = ld_sc1(rs, x0 + p * XSTR);
        const bool ok = (v[1] == tag) & (v[3] == tag) & (v[0] == ~v[2]);
        e += ok ? __uint_as_float(v[0]) : 0.f; c += ok;
      }
      if (c == np) break;
    }
    s += e;
  }
  return s;
}
struct FxC { double up, dn; float hi, lo, rg; };
DI FxC fx_const() { FxC c = {0x1.8p52, -0x1.4p12, 16.f, 0x1p-24f, 1024.f}; PIN(c.up); PIN(c.dn); asm volatile("" : "+s"(c.hi), "+s"(c.lo), "+s"(c.rg)); return c; }
template <int NARR> DI u64 fx_add2(char* ws, __amdgpu_buffer_rsrc_t rs, float y, u32 aoff, u32 xown, u32 tag, const FxC& c) {
  const float t = __builtin_fabsf(y);
  u64 x = __builtin_bit_cast(u64, __builtin_fma((double)y, 0x1p40, c.up)) + (0x0400000000000000ull - 0x4338000000000000ull);
  PIN(x);
  const u64 big = __ballot(!(t < c.hi)), some = __ballot(t >= c.lo);
  u64 odd;
  asm("s_cmp_eq_u64 %2, 0\n\ts_cselect_b64 %0, -1, %1" : "=s"(odd) : "s"(big), "s"(some) : "scc");
  if (__builtin_expect(odd != 0ull, 0)) {
    if (__ballot(t != 0.f) != 0ull) {
      st_sc1(rs, xown, u32x4{__float_as_uint(y), tag, ~__float_as_uint(y), tag});
      x = 0x0408000000000000ull;
      asm volatile("s_waitcnt vmcnt(0)" ::: "memory");
    }
  }
  const u64 o = __hip_atomic_fetch_add(reinterpret_cast<u64*>(ws + aoff), x, __ATOMIC_RELAXED, __HIP_MEMORY_SCOPE_AGENT);
  SCHED_FENCE();
  x += (0x433ull << 52) - ((u64)NARR << 58);
  PIN(x);
  return o + x;
}
DI float fx_dec(u64 d, double dn) { return (float)__builtin_fma(__builtin_bit_cast(double, d), 0x1p-40, dn); }
template <int NARR> DI u64 fx_raw(u64 d) { return d - ((0x433ull << 52) - ((u64)NARR << 58)); }
template <int NARR> DI void fx_out(const char* out, __amdgpu_buffer_rsrc_t rs, u64 d, u32 ooff, u32 x0, u32 tag, const FxC& c) {
  float s = fx_dec(d, c.dn);
  if (__builtin_fabsf(s) < c.rg) *reinterpret_cast<u16*>(const_cast<char*>(out) + ooff) = (u16)pk2(s, s);
  SCHED_FENCE();
  PIN(s);
  const u64 t = fx_raw<NARR>(d);
  if (__builtin_expect(!(__builtin_fabsf(s) < c.rg) & ((u32)(t >> 58) == (NARR & 63)), 0)) { const float e = fx_val<NARR, 32768>(rs, t, x0, tag); *reinterpret_cast<u16*>(const_cast<char*>(out) + ooff) = (u16)pk2(e, e); }
}
template <int CTRL, int BANK> DI u32 dppb(u32 old, u32 v) { return (u32)__builtin_amdgcn_update_dpp((int)old, (int)v, CTRL, 0xf, BANK, false); }
constexpr u32 OOB = 0x80000000u;
template <int N>
DI void row_issue(__amdgpu_buffer_rsrc_t rs, const u32 (&off)[N], u32x4 (&v)[N]) {
#pragma unroll
  for (int p = 0; p < N; ++p) v[p] = ld_sc1(rs, off[p]);
}
template <int N>
DI void row_wait(__amdgpu_buffer_rsrc_t rs, const u32 (&off)[N], u32x4 (&v)[N], u32 tag, bool act, int lane) {
  bool ok = true;
#pragma unroll
  for (int p = 0; p < N; ++p) PIN(v[p]);
#pragma unroll
  for (int p = 0; p < N; ++p) ok &= (v[p][3] == tag);
  if (__builtin_expect(!__all(ok | !act), 0)) {
#pragma unroll 1
    for (u32 spins = 0; spins < SPIN_CAP; ++spins) {
      __builtin_amdgcn_s_sleep(2);
      asm volatile("" ::: "memory");
      u32x4 t[N]; bool re[N];
#pragma unroll
      for (int p = 0; p < N; ++p) {
        const u64 m = __ballot(act & !((v[p][3] == tag)));
        re[p] = ((u32)(m >> (lane & 48)) & 0xffffu) != 0u;
        t[p] = ld_sc1(rs, re[p] ? off[p] : OOB);
      }
      bool k2 = true;
#pragma unroll
      for (int p = 0; p < N; ++p) { v[p] = re[p] ? t[p] : v[p]; PIN(v[p]); k2 &= (v[p][3] == tag); }
      if (__all(k2 | !act)) break;
    }
  }
}
template <int W> DI void window() { asm volatile("s_waitcnt vmcnt(%0)" :: "n"(W - 1) : "memory"); SCHED_FENCE(); }
template <int W> DI void soft(u32 org, int ns) {
  u64 t; u32 o;
  asm volatile("s_memrealtime %0\n\ts_waitcnt lgkmcnt(0)" : "=s"(t) :: "memory");
  asm volatile("s_sub_i32 %0, %1, %4\n\ts_cmp_gt_i32 %0, %2\n\ts_cbranch_scc1 1f\n\ts_waitcnt vmcnt(%3)\n1:" : "=&s"(o) : "s"((u32)t), "n"(ns / 10), "n"(W - 1), "s"(org) : "memory", "scc");
  SCHED_FENCE();
}
DI u32 clk32() { return (u32)__builtin_amdgcn_s_memrealtime(); }
DI void hint_wait(__amdgpu_buffer_rsrc_t rs, u32 off, u32x2 hv, u32 tag, int dt) {
  PIN(hv);
  if (__builtin_expect(!__all(hv[1] == tag), 0)) {
#pragma unroll 1
    for (u32 spins = 0; spins < 8; ++spins) {
      asm volatile("" ::: "memory");
      const u32x2 t = ld_sc1_64(rs, hv[1] != tag ? off : OOB);
      hv = hv[1] != tag ? t : hv;
      PIN(hv);
      if (__all(hv[1] == tag)) break;
    }
  }
  const u32 t0 = clk32();
  const float d = row16_maxq(hv[1] == tag ? (float)(int)(hv[0] - t0) : -1000.f);
  const int n = min(__builtin_amdgcn_readfirstlane((int)d) + dt, 250);
#pragma unroll 1
  for (int i = 0; i < 256 && (int)(clk32() - t0) < n; ++i) __builtin_amdgcn_s_sleep(1);
  asm volatile("" ::: "memory");
}
DI u32 next_tag(u32 e) { return e * 1664525u + 1013904223u; }

DI void s1_mfma(const u32x4 (&R)[2][8], const u32x4 (&xf)[2], int r, f32x4& s0, f32x4& s1) {
  f32x4 acc[8];
#pragma unroll
  for (int j = 0; j < 8; ++j) acc[j] = mfma16(asbf(xf[0]), bfrag(R[0], j), f32x4{0.f, 0.f, 0.f, 0.f});
#pragma unroll
  for (int j = 0; j < 8; ++j) PIN(acc[j]);
  SCHED_FENCE();
#pragma unroll
  for (int j = 0; j < 8; ++j) acc[j] = mfma16(asbf(xf[1]), bfrag(R[1], j), acc[j]);
  s0 = sel4(acc[0], acc[2], acc[4], acc[6], r); s1 = sel4(acc[1], acc[3], acc[5], acc[7], r);
}
DI int qoffs(int qd, int rot) { const int aq = (qd + rot) & 3; return 32 * (aq >> 1) + 4 * (aq & 1); }
template <class F> DI void s1_load(const u16* wsrc, const u16* xsrc, int rot, u32x4 (&R)[2][8], u32x4 (&xf)[2], F mid) {
#pragma unroll
  for (int k = 0; k < 2; ++k) {
    const u32x2 lo = ldnt8(xsrc + qoffs(2 * k, rot)), hi = ldnt8(xsrc + qoffs(2 * k + 1, rot));
    xf[k] = u32x4{lo[0], lo[1], hi[0], hi[1]};
  }
#pragma unroll
  for (int qd = 0; qd < 4; ++qd)
#pragma unroll
    for (int i = 0; i < 4; ++i) {
      if (i == 0 && qd == 2) mid();
      if (i == 0 && qd > 0) { SCHED_FENCE(); __builtin_amdgcn_s_barrier(); SCHED_FENCE(); }
      if (qd == 0) window<WSB>();
      R[qd >> 1][4 * (qd & 1) + i] = ldsh(wsrc + (size_t)(qoffs(qd, rot) + i) * (NH * DH));
    }
}

DI float score4(const u32x4 (&k)[4], const u32x4& qpk, bool hi8, bool hi4) {
  const float d0 = dot8(k[0], qpk), d1 = dot8(k[1], qpk), d2 = dot8(k[2], qpk), d3 = dot8(k[3], qpk);
  const float a0 = (hi8 ? d2 : d0) + dppf<0x128>(hi8 ? d0 : d2), a1 = (hi8 ? d3 : d1) + dppf<0x128>(hi8 ? d1 : d3);
  float s = (hi4 ? a1 : a0) + dppf<0x141>(hi4 ? a0 : a1);
  s += dppf<0xB1>(s); s += dppf<0x4E>(s);
  return s * SCL2;
}
DI u32 qtag(const u32x4& v) { return __builtin_amdgcn_perm(__builtin_amdgcn_perm(v[3], v[2], 0x04000400u), __builtin_amdgcn_perm(v[1], v[0], 0x04000400u), 0x05040100u); }
DI void q_finish(__amdgpu_buffer_rsrc_t rs, u32 off, u32x4 v, u32 tag, char* dst, int lane) {
  PIN(v);
  if (__builtin_expect(!__all(qtag(v) == tag), 0)) {
#pragma unroll 1
    for (u32 spins = 0; spins < SPIN_CAP; ++spins) {
      asm volatile("" ::: "memory");
      v = ld_sc1(rs, off);
      PIN(v);
      if (__all(qtag(v) == tag)) break;
      __builtin_amdgcn_s_sleep(1);
    }
  }
  float q[4];
#pragma unroll
  for (int k = 0; k < 4; ++k) q[k] = x32_sum(x16_sum(__uint_as_float(v[k] & ~255u)));
  if (lane < 16) reinterpret_cast<u32x2*>(dst)[lane] = u32x2{pk2(q[0], q[1]), pk2(q[2], q[3])};
}

DI void roleB(const KArgs& a, char* smem, const int g, const int idx, const int w, const int lane, const u32x4 (&uk)[4], const u32 tvo, const u32 cpf, const u64 ts0_) {
  constexpr int ROLE_ = 2;
  TS_DECL; TS(0);
  const int tid = w * 64 + lane, r = lane >> 4, c16 = lane & 15;
  const int h = 4 * (idx & 7) + ((idx >> 3) & 3), e4 = idx >> 5;
  const int b3 = (idx >> 1) & 3, hh = (idx >> 3) & 1, kp = (idx & 1) * 8 + (idx >> 4);
  const __amdgpu_buffer_rsrc_t rs = __builtin_amdgcn_make_buffer_rsrc((void*)a.ws, 0, (int)WS_END, RSRC_W3);
  u32 tag = *reinterpret_cast<const u32*>(a.ws + WS_EPOCH);
  u32x4 tv[32], uv[4], ow[4];
  const u16* owp = a.wob + (size_t)(h * DH + 16 * w) * DM + 128 * e4;
  const u16* uvp = a.wuv + ((size_t)(128 * e4 + 16 * w) * NH + h) * DH;
  u32 owo = (4 * r * DM + 8 * c16) * 2, uvo = (4 * r * NH * DH + 8 * c16) * 2;
  const u32 tso = tvo ^ (128 * DC * 2);
  auto rows = [&](int lo, int hi) {
#pragma unroll
    for (int n = 0; n < 32; ++n)
      if (n >= lo && n < hi && !(n & 8)) {
        if (n >= NB0 && n < 8) window<WSB>();
        tv[n] = ldsh(reinterpret_cast<const u16*>(reinterpret_cast<const char*>(a.ckv) + ((n >> 1 & 8) + (n & 7)) * (8 * DC * 2) + tvo));
        SCHED_FENCE();
      }
#pragma unroll
    for (int n = 0; n < 32; ++n)
      if (n >= lo && n < hi && (n & 8)) { tv[n] = ldnt(reinterpret_cast<const u16*>(reinterpret_cast<const char*>(a.ckv) + ((n >> 1 & 8) + (n & 7)) * (8 * DC * 2) + tso)); SCHED_FENCE(); }
  };
  u32 atw[2];
#pragma unroll
  for (int k = 0; k < 2; ++k) atw[k] = L_T + (lane >> 3) * 16384 + w * 128 + (((lane & 7) ^ fsw(8 * k + w)) << 4);
  auto tile = [&](int lo, int hi) {
#pragma unroll
    for (int n = 0; n < 32; ++n) if (n >= lo && n < hi) *reinterpret_cast<LDSP u32x4*>((LDSP char*)atw[n & 1] + (n & 15) * 1024) = tv[n];
    SCHED_FENCE();
  };
  u32x4 ua[4];
  {
    const int m0 = 64 * w + 8 * r;
    const u16* wsrc = a.wqb + (size_t)m0 * (NH * DH) + h * DH + 8 * c16;
    const u16* xsrc = a.xb + (c16 & 3) * DM + m0;
    u32x4 R[2][8], xf[2];
    if (w >= 4 && hh != (h >> 4)) st_sc1(rs, WS_ACC + (u32)((((idx & 7) | ((idx >> 4) << 3)) * 4 + w - 4) * 64 + lane) * 16, u32x4{0u, 0x40000u, 0u, 0x40000u});
    SCHED_FENCE();
    rows(0, 8);
    auto early = [&]() {
      SCHED_FENCE();
      asm volatile("" :: "v"(cpf));
      PIN(tag); tag = next_tag(tag);
      char* us = smem + L_UKT + w * 16384;
#pragma unroll
      for (int i = 0; i < 4; ++i) *reinterpret_cast<u32x4*>(us + (4 * r + i) * 256 + ((c16 ^ (4 * r + i)) << 4)) = uk[i];
      asm volatile("" ::: "memory");
#pragma unroll
      for (int i = 0; i < 4; ++i) ua[i] = *reinterpret_cast<const u32x4*>(us + c16 * 256 + (((4 * i + r) ^ c16) << 4));
#pragma unroll
      for (int i = 0; i < 4; ++i) PIN(ua[i]);
      SCHED_FENCE();
      PIN(atw[0]); PIN(atw[1]);
      tile(0, 8);
    };
    s1_load(wsrc, xsrc, e4, R, xf, early);
    SCHED_FENCE();
    rows(8, 16);
    f32x4 s0, s1;
    s1_mfma(R, xf, r, s0, s1);
    float* pp = reinterpret_cast<float*>(smem + L_S1P) + w * 4096 + 8 * c16 + 2 * r;
#pragma unroll
    for (int b = 0; b < NB; ++b) *reinterpret_cast<u32x2*>(pp + b * DH) = u32x2{__float_as_uint(s0[b]), __float_as_uint(s1[b])};
  }
  wg_barrier();
  SCHED_FENCE();
  rows(16, N3);
  {
    const float* pp = reinterpret_cast<const float*>(smem + L_S1P) + tid;
    const float s = ((pp[0] + pp[4096]) + (pp[8192] + pp[12288])) + ((pp[16384] + pp[20480]) + (pp[24576] + pp[28672]));
    reinterpret_cast<u16*>(smem + L_QB)[tid] = (u16)pk2(s, s);
  }
  wg_barrier();
  SCHED_FENCE();
  {
    f32x4 d0 = f32x4{0.f, 0.f, 0.f, 0.f};
#pragma unroll
    for (int i = 0; i < 4; ++i) {
      const bf16x8 qf = *reinterpret_cast<const bf16x8*>(smem + L_QB + ((c16 & 3) * DH + 32 * i + 8 * r) * 2);
      d0 = mfma16(asbf(ua[i]), qf, d0);
    }
    if (c16 < 4) {
      char* o = smem + L_QO + w * 256 + c16 * 48;
      *reinterpret_cast<u32*>(o + (r < 3 ? 16 * r : 8)) = pk2(d0[0], d0[1]);
      *reinterpret_cast<u32*>(o + (r < 3 ? 16 * r + 4 : 24)) = pk2(d0[2], d0[3]);
    }
    SCHED_FENCE();
    tile(8, 16);
    wg_barrier();
    if (w < 4 && lane < 24) {
      u32x4 x = *reinterpret_cast<const u32x4*>(smem + L_QO + (lane / 3) * 256 + (3 * w + lane % 3) * 16);
      x[3] = tag; x[2] = lane % 3 == 2 ? x[1] : x[2];
      st_sc1(rs, WS_QP + (u32)(((w * 4 + e4) * 32 + h) * 24 + lane) * 16, x);
    }
  }
  SCHED_FENCE();
  TS(2);
  rows(N3, 24);
  constexpr int QN = 3;
  u32x4 qv[QN]; u32 qoff[QN], pa64[QN], pa32[QN];
#pragma unroll
  for (int k = 0; k < QN; ++k) { qoff[k] = WS_QP + (u32)((b3 * 8 + (w & 6) + hh) * 384 + 192 * (w & 1) + 64 * k + lane) * 16; PIN(qoff[k]); }
  u32 ack[2][2], atf[4];
  {
    const u32 rho = 32 * (w >> 1) + 8 * (c16 >> 2) + 4 * (w & 1) + (c16 & 3), tq = c16 >> 2, tp = lane & 3, rr = 8 * r + tq;
#pragma unroll
    for (int k = 0; k < 4; ++k) { ack[k >> 1][k & 1] = L_T + (k >> 1) * 65536 + rho * 128 + (((4 * (k & 1) + r) ^ fsw(rho)) << 4); PIN(ack[k >> 1][k & 1]); }
#pragma unroll
    for (int ct = 0; ct < 4; ++ct) { atf[ct] = L_T + w * 16384 + rr * 128 + (((2 * ct + (tp >> 1)) ^ fsw(rr)) << 4) + 8 * (tp & 1); PIN(atf[ct]); }
  }
  u32x4 ck[2][16];
  bf16x8 tf[2][16];
  auto ckread = [&](int t, int s) { ck[t][s] = *reinterpret_cast<const LDSP u32x4*>((const LDSP char*)ack[s >> 3][s & 1] + ((s >> 1) & 3) * 16384); };
  auto tfread = [&](int t, int lo, int hi) {
#pragma unroll
    for (int k = 0; k < 16; ++k) {
      if (k < lo || k >= hi) continue;
      s16x4 b0 = __builtin_amdgcn_ds_read_tr16_b64_v4i16((LDSP s16x4*)((LDSP char*)atf[k & 3] + 4096 * (k >> 2)));
      s16x4 b1 = __builtin_amdgcn_ds_read_tr16_b64_v4i16((LDSP s16x4*)((LDSP char*)atf[k & 3] + 4096 * (k >> 2) + 512));
      tf[t][k] = __builtin_shufflevector(b0, b1, 0, 1, 2, 3, 4, 5, 6, 7);
    }
  };
#pragma unroll
  for (int s = 0; s < 16; ++s) ckread(0, s);
  tfread(0, 0, 16);
  SCHED_FENCE();
#pragma unroll
  for (int k = 0; k < QN; ++k) {
    const int G = 192 * (w & 1) + 64 * k + lane, hl = G / 24, rec = G % 24, u = rec / 3, j = rec - 3 * u, sb = 16 * (w >> 1) + 2 * u;
    pa64[k] = L_Q + hl * 1024 + (((sb + (j >> 1)) ^ hl) << 4) + 8 * (j & 1);
    pa32[k] = L_Q + hl * 1024 + (((sb + 1) ^ hl) << 4) + (j == 2 ? 4 : 8 + 4 * j);
    PIN(pa64[k]); PIN(pa32[k]);
  }
  u32 aq = L_Q + c16 * 1024 + ((r ^ c16) << 4);
  PIN(aq); PIN(uvo);
#pragma unroll
  for (int s = 0; s < 16; ++s) { PIN(ck[0][s]); PIN(tf[0][s]); }
  SCHED_FENCE();
  TS(15);
  wg_barrier();
  SCHED_FENCE();
  rows(24, 24 + SP0);
#pragma unroll
  for (int n = 16; n < 32; ++n) {
    tile(n, n + 1);
    if (n + 8 + SP0 == 32) { row_issue<QN>(rs, qoff, qv); SCHED_FENCE(); }
    rows(n + 8 + SP0, n + 9 + SP0);
  }
  TS(14);
  u32 amx = L_MX + c16 * 32, asm_ = L_SM + c16 * 48 + 4 * w, apw = L_P + (w >> 1) * 1024 + lane * 16 + (w & 1) * 8, apf = L_P + lane * 16;
  u32 asr = L_SM + (4 * r + (c16 & 3)) * 48;
  u32 gb = WS_PART + (u32)((b3 * 16 + kp) * 8 + w) * PBLK, gm = c16 < 4 ? gb + 4096 + (16 * hh + 4 * r + c16) * 16 : OOB;
  gb += hh * 2048 + r * 512 + c16 * 16;
  u32 aqx[4];
#pragma unroll
  for (int k = 0; k < 4; ++k) { aqx[k] = aq ^ (u32)(64 * k); PIN(aqx[k]); }
  PIN(amx); PIN(asm_); PIN(apw); PIN(apf); PIN(asr); PIN(gb); PIN(gm);
  SCHED_FENCE();
  row_wait<QN>(rs, qoff, qv, tag, true, lane);
  SCHED_FENCE(); TS(16); SCHED_FENCE();
#pragma unroll
  for (int k = 0; k < QN; ++k) {
    *reinterpret_cast<LDSP u32x2*>((LDSP char*)pa64[k]) = u32x2{qv[k][0], qv[k][1]};
    *reinterpret_cast<LDSP u32*>((LDSP char*)pa32[k]) = qv[k][2];
  }
  SCHED_FENCE();
#pragma unroll
  for (int n = 0; n < 4; ++n) uv[n] = ldnt(reinterpret_cast<const u16*>(reinterpret_cast<const char*>(uvp + (size_t)n * (NH * DH)) + uvo));
  SCHED_FENCE();
  wg_barrier();
  SCHED_FENCE();
  TS(17);
  if (w == 7) { const u32 t = clk32(); if (lane == 0) st_sc1(rs, WS_FLG + (u32)idx * 128, u32x4{tag, tag, t, tag}); }
  const u32 hco = WS_FLG + (u32)((w & 3) * 2 + (c16 >> 3) + 16 * (c16 & 7) + 8 * (h >> 4)) * 128 + 8;
  u32 cp2 = 0;
  u32x2 hc;
  SCHED_FENCE();
  {
    f32x4 sc[2];
    u32x4 qa[16];
    constexpr int QA = 4;
    auto qread = [&](int s) { qa[s] = *reinterpret_cast<const LDSP u32x4*>((const LDSP char*)aqx[s & 3] + (s >> 2) * 256); ckread(1, s); };
#pragma unroll
    for (int s = 0; s < QA; ++s) qread(s);
#pragma unroll
    for (int s = 0; s < 16; ++s) {
      SCHED_FENCE();
      if (s + QA < 16) qread(s + QA);
      SCHED_FENCE();
#pragma unroll
      for (int t = 0; t < 2; ++t) sc[t] = mfma16(asbf(ck[t][s]), asbf(qa[s]), s ? sc[t] : f32x4{0.f, 0.f, 0.f, 0.f});
    }
    SCHED_FENCE();
    {
      float m = fmaxf(fmaxf(fmaxf(sc[0][0], sc[0][1]), fmaxf(sc[0][2], sc[0][3])), fmaxf(fmaxf(sc[1][0], sc[1][1]), fmaxf(sc[1][2], sc[1][3])));
      m = x32_max(x16_max(m));
      *reinterpret_cast<LDSP float*>((LDSP char*)amx + 4 * w) = m;
      SCHED_FENCE(); TS(4); SCHED_FENCE();
      wg_barrier();
      SCHED_FENCE(); TS(18); SCHED_FENCE();
      const f32x4 m0 = *reinterpret_cast<const LDSP f32x4*>((const LDSP char*)amx), m1 = *reinterpret_cast<const LDSP f32x4*>((const LDSP char*)amx + 16);
      m = fmaxf(fmaxf(fmaxf(m0[0], m0[1]), fmaxf(m0[2], m0[3])), fmaxf(fmaxf(m1[0], m1[1]), fmaxf(m1[2], m1[3]))) * SCL2;
      u32x2 p0, p1;
      p0[0] = pk2(__builtin_amdgcn_exp2f(sc[0][0] * SCL2 - m), __builtin_amdgcn_exp2f(sc[0][1] * SCL2 - m));
      p0[1] = pk2(__builtin_amdgcn_exp2f(sc[0][2] * SCL2 - m), __builtin_amdgcn_exp2f(sc[0][3] * SCL2 - m));
      p1[0] = pk2(__builtin_amdgcn_exp2f(sc[1][0] * SCL2 - m), __builtin_amdgcn_exp2f(sc[1][1] * SCL2 - m));
      p1[1] = pk2(__builtin_amdgcn_exp2f(sc[1][2] * SCL2 - m), __builtin_amdgcn_exp2f(sc[1][3] * SCL2 - m));
      *reinterpret_cast<LDSP u32x2*>((LDSP char*)apw) = p0;
      *reinterpret_cast<LDSP u32x2*>((LDSP char*)apw + 4096) = p1;
      SCHED_FENCE();
      const float ls = x32_sum(x16_sum(dot2(p0[1], 0x3f803f80u, dot2(p0[0], 0x3f803f80u, 0.f)) + dot2(p1[1], 0x3f803f80u, dot2(p1[0], 0x3f803f80u, 0.f))));
      *reinterpret_cast<LDSP float*>((LDSP char*)asm_) = ls;
      *reinterpret_cast<LDSP float*>((LDSP char*)asm_ + 32 - 4 * w) = m;
    }
    wg_barrier();
    SCHED_FENCE(); TS(5); SCHED_FENCE();
    u32x4 pf[8];
#pragma unroll
    for (int k = 0; k < 4; ++k) pf[k] = *reinterpret_cast<const LDSP u32x4*>((const LDSP char*)apf + k * 1024);
    SCHED_FENCE();
    hc = ld_sc1_64(rs, hco);
    SCHED_FENCE();
    f32x4 acc[4], l0, l1;
    u32 lw = 0, c0 = 0, mw = 0;
#pragma unroll
    for (int n = 0; n < 32; ++n) {
      acc[n & 3] = mfma16(asbf(pf[n >> 2]), tf[n >> 4][n & 15], n < 4 ? f32x4{0.f, 0.f, 0.f, 0.f} : acc[n & 3]);
      SCHED_FENCE();
      if (n < 4) pf[4 + n] = *reinterpret_cast<const LDSP u32x4*>((const LDSP char*)apf + (4 + n) * 1024);
      tfread(1, n, n + 1);
      if (n == 16) {
        l0 = *reinterpret_cast<const LDSP f32x4*>((const LDSP char*)asr); l1 = *reinterpret_cast<const LDSP f32x4*>((const LDSP char*)asr + 16);
        mw = *reinterpret_cast<const LDSP u32*>((const LDSP char*)asr + 32);
      }
      if (n == 20 && w == 6) {
        u64 pc; asm volatile("s_getpc_b64 %0" : "=s"(pc));
        cp2 = *reinterpret_cast<const __attribute__((address_space(1))) u32*>((pc & ~127ull) + min(lane, CPL2) * 128);
      }
      if (n == 24) { lw = __float_as_uint(((l0[0] + l0[1]) + (l0[2] + l0[3])) + ((l1[0] + l1[1]) + (l1[2] + l1[3]))); c0 = tag + mw + lw; PIN(lw); PIN(c0); }
      SCHED_FENCE();
    }
    SCHED_FENCE(); TS(6); SCHED_FENCE();
    u32 cs[2];
#pragma unroll
    for (int ip = 0; ip < 2; ++ip) {
      const u32 a0 = pk2(acc[0][2 * ip], acc[1][2 * ip]), a1 = pk2(acc[2][2 * ip], acc[3][2 * ip]);
      const u32 b0 = pk2(acc[0][2 * ip + 1], acc[1][2 * ip + 1]), b1 = pk2(acc[2][2 * ip + 1], acc[3][2 * ip + 1]);
      const u32x4 x = u32x4{dppb<0x128, 0xc>(a0, b0) + tag, dppb<0x128, 0xc>(a1, b1), dppb<0x128, 0x3>(b0, a0), dppb<0x128, 0x3>(b1, a1)};
      st_sc1(rs, gb + ip * 256, x);
      cs[ip] = (x[0] + x[1]) + (x[2] + x[3]);
    }
    {
      const bool o2 = c16 & 2;
      u32 c = (o2 ? cs[1] : cs[0]) + dppu<0x141>(o2 ? cs[0] : cs[1]);
      c += dppu<0xB1>(c);
      c += dppb<0x124, 0xa>(dppb<0x12c, 0x5>(0u, c), c);
      c = (c16 & 1) ? dppu<0x128>(c) : c;
      st_sc1(rs, gm, u32x4{mw, lw, c + c0, tag});
    }
    SCHED_FENCE(); TS(19); SCHED_FENCE();
  }
  bf16x8 uvf[4];
#pragma unroll
  for (int n = 0; n < 4; ++n) {
    const int c = 16 * w + 4 * r + n;
    *reinterpret_cast<u32x4*>(smem + L_T + w * 16384 + (4 * r + n) * 256 + ((c16 ^ (2 * (((c >> 4) & 3) | ((c & 1) << 2)))) << 4)) = uv[n];
  }
  wg_barrier();
  SCHED_FENCE();
  TS(20);
  u32 lrd = L_LAT + ((c16 & 4) ? 4 + (~c16 & 3) : (c16 & 3)) * 272 + 16 * r;
  const u32 word = 512 * e4 + tid;
  u32 cxa = WS_ACX + (u32)(h * 512 + tid) * 8, exw = L_EX + (w * 128 + 8 * c16 + ((2 * r) ^ (c16 & 7))) * 16;
  u32 aoff = WS_ACC + 65536 + (word >> 3) * 256 + (word & 7) * 8, ooff;
  { const u32 col = (tid >> 2) ^ ((tid >> 5) & 7); ooff = (((col & 2) ? (~tid & 3) : (tid & 3)) * 1024 + 512 + 128 * e4 + col) * 2; }
  u32 mk[4], mq[4], m4 = (lane & 4) ? ~0u : 0u, m8 = (lane & 8) ? ~0u : 0u, mh = (lane & 4) ? 0xffff0000u : 0xffffu, lrx = L_EX + tid * 4, tag9 = 9 * tag;
#pragma unroll
  for (int k = 0; k < 4; ++k) { mk[k] = ((lane & 7) >> 1) == k ? ((lane & 1) ? 0xffff0000u : 0xffffu) : 0u; mq[k] = (c16 >> 2) == k ? ~0u : 0u; PIN(mk[k]); PIN(mq[k]); }
  PIN(m4); PIN(m8); PIN(mh); PIN(lrx); PIN(tag9);
  const FxC fc = fx_const(), fd = fx_const();
  u64 owb = (u64)owp;
  asm volatile("" : "+s"(owb));
  {
    const u32 pblk = WS_PART + (u32)(((w & 3) * 16 + (lane >> 3)) * 8 + 2 * e4 + (w >> 2)) * PBLK;
    u32 off[2], moff = pblk + (lane & 4) * (16 * PBLK) + 4096 + h * 16; u32x4 v[2], mv;
#pragma unroll
    for (int t = 0; t < 2; ++t) { off[t] = pblk + h * 128 + ((lane & 7) << 4) + (u32)(t * 64 * PBLK); PIN(off[t]); }
    PIN(moff); PIN(owo);
    if (idx == 0 && w == 7) {
      const u32 eoff = WS_QX + (u32)((lane >> 2) * 16 + (lane & 3)) * DH * 4;
#pragma unroll 1
      for (u32 spins = 0; spins < SPIN_CAP; ++spins) {
        const u32x4 e0 = ld_sc1(rs, eoff), e1 = ld_sc1(rs, eoff + 16), e2 = ld_sc1(rs, eoff + 16 * 16 * DH * 4), e3 = ld_sc1(rs, eoff + 16 * 16 * DH * 4 + 16), e4_ = ld_sc1(rs, WS_FLG + 8 * 128);
        if (__all((qtag(e0) == tag) & (qtag(e1) == tag) & (qtag(e2) == tag) & (qtag(e3) == tag) & (e4_[0] == tag) & (e4_[1] == tag))) break;
        asm volatile("" ::: "memory");
      }
      if (lane == 0) st_sc1_32(rs, WS_EPOCH, tag);
    }
    hint_wait(rs, hco, hc, tag, THC);
    asm volatile("" ::: "memory");
    TS(7);
    mv = ld_sc1(rs, moff);
    SCHED_FENCE();
    row_issue<2>(rs, off, v);
    SCHED_FENCE();
#pragma unroll
    for (int s = 0; s < 4; ++s) {
      const u32 tq = c16 >> 2, tp = lane & 3, sw = 2 * (tq | (4 * (r & 1)));
      const char* p = smem + L_T + (4 * (s >> 1) + tq) * 16384 + (4 * (s & 1) + r) * 256 + (((2 * w + (tp >> 1)) ^ sw) << 4) + 8 * (tp & 1);
      s16x4 b0 = __builtin_amdgcn_ds_read_tr16_b64_v4i16((LDSP s16x4*)p);
      s16x4 b1 = __builtin_amdgcn_ds_read_tr16_b64_v4i16((LDSP s16x4*)(p + 8 * 256));
      uvf[s] = __builtin_shufflevector(b0, b1, 0, 1, 2, 3, 4, 5, 6, 7);
      PIN(uvf[s]);
    }
    u32 lad = (c16 >> 3) == (r >> 1) ? L_LAT + (4 * (r >> 1) + (w & 3)) * 272 + (w >> 2) * 128 + (c16 & 7) * 16 + (r & 1) * 8 : L_LAT + 4096 + tid * 8;
    PIN(lrd);
    PIN(lad); PIN(ooff); PIN(aoff); PIN(cxa); PIN(exw);
    SCHED_FENCE();
    auto comb = [&](bool first) {
      float M, T;
      asm volatile("v_max_f32_dpp %0, %2, %2 row_half_mirror row_mask:0xf bank_mask:0xf\n\ts_nop 1\n\tv_max_f32_dpp %0, %0, %0 row_ror:8 row_mask:0xf bank_mask:0xf\n\t"
                   "v_mov_b32 %1, %0\n\ts_nop 1\n\tv_permlane16_swap_b32 %0, %1\n\tv_max_f32 %0, %0, %1\n\t"
                   "v_mov_b32 %1, %0\n\ts_nop 1\n\tv_permlane32_swap_b32 %0, %1\n\tv_max_f32 %0, %0, %1" : "=&v"(M), "=&v"(T) : "v"(mv[0]));
      const float ex = __builtin_amdgcn_exp2f(__uint_as_float(mv[0]) - M);
      const u32 eb = pk2(ex, ex), a0 = eb & mk[0], a1 = eb & mk[1];
      SCHED_FENCE();
      u32 eo = dppu<0x141>(eb);
      PIN(eo);
      SCHED_FENCE();
      const u32 b2 = eb & mk[2], b3 = eb & mk[3];
      SCHED_FENCE();
      const f32x4 d0 = mfma16(asbf(u32x4{a0, a1, eo & mk[2], eo & mk[3]}), asbf(v[0]), f32x4{0.f, 0.f, 0.f, 0.f});
      const f32x4 d = mfma16(asbf(u32x4{eo & mk[0], eo & mk[1], b2, b3}), asbf(v[1]), d0);
      SCHED_FENCE();
      if (first) {
#pragma unroll
        for (int n = 0; n < 4; ++n) ow[n] = __builtin_nontemporal_load(reinterpret_cast<const __attribute__((address_space(1))) u32x4*>(owb + n * (DM * 2) + owo));
      }
      SCHED_FENCE();
      u32 rl, s0, s1, t0, t1;
      asm volatile("v_and_b32 %0, 0xffff0000, %5\n\tv_add_u32 %1, %10, %11\n\tv_mul_f32 %0, %0, %7\n\tv_add_u32 %2, %14, %15\n\tv_add_u32 %3, %12, %13\n\t"
                   "v_add_f32_dpp %0, %0, %0 row_half_mirror row_mask:0xf bank_mask:0xf\n\tv_add_u32 %4, %16, %17\n\tv_add_u32 %1, %1, %3\n\t"
                   "v_add_f32_dpp %0, %0, %0 row_ror:8 row_mask:0xf bank_mask:0xf\n\tv_add_u32 %2, %2, %4\n\tv_mov_b32 %3, %0\n\t"
                   "v_bitop3_b32 %4, %18, %2, %1 bitop3:0xca\n\tv_bitop3_b32 %1, %18, %1, %2 bitop3:0xca\n\t"
                   "v_permlane16_swap_b32 %0, %3\n\tv_add_f32 %0, %0, %3\n\t"
                   "v_add_u32_dpp %1, %1, %4 row_half_mirror row_mask:0xf bank_mask:0xf\n\tv_mov_b32 %3, %0\n\tv_add_u32 %2, %6, %7\n\t"
                   "v_add_u32_dpp %1, %1, %1 quad_perm:[1,0,3,2] row_mask:0xf bank_mask:0xf\n\t"
                   "v_permlane32_swap_b32 %0, %3\n\tv_add_f32 %0, %0, %3\n\t"
                   "v_add_u32_dpp %1, %1, %1 quad_perm:[2,3,0,1] row_mask:0xf bank_mask:0xf\n\t"
                   "v_rcp_f32 %0, %0\n\tv_add_u32 %2, %2, %20\n\tv_sub_u32 %2, %2, %8\n\tv_add_u32 %1, %1, %2\n\t"
                   "v_mul_f32 %2, %21, %0\n\tv_mul_f32 %3, %22, %0\n\tv_mul_f32 %4, %23, %0\n\tv_mul_f32 %0, %24, %0\n\t"
                   "v_cvt_pk_bf16_f32 %2, %2, %3\n\tv_cvt_pk_bf16_f32 %3, %4, %0"
                   : "=&v"(rl), "=&v"(s0), "=&v"(s1), "=&v"(t0), "=&v"(t1)
                   : "v"(eb), "v"(mv[0]), "v"(mv[1]), "v"(mv[2]), "v"(mv[3]), "v"(v[0][0]), "v"(v[0][1]), "v"(v[0][2]), "v"(v[0][3]),
                     "v"(v[1][0]), "v"(v[1][1]), "v"(v[1][2]), "v"(v[1][3]), "v"(m4), "v"(d0), "v"(tag9), "v"(d[0]), "v"(d[1]), "v"(d[2]), "v"(d[3]));
      *reinterpret_cast<LDSP u32x2*>((LDSP char*)lad) = u32x2{s1, t0};
      SCHED_FENCE();
      return __ballot(__builtin_amdgcn_bitop3_b32(s0, mv[3], tag, 0xf6) != 0u);
    };
    asm volatile("" :: "v"(cp2));
#pragma unroll
    for (int t = 0; t < 2; ++t) v[t][0] -= tag;
    u64 bm = comb(true);
    if (__builtin_expect(bm != 0ull, 0)) {
#pragma unroll 1
      for (u32 spins = 0; spins < SPIN_CAP; ++spins) {
        __builtin_amdgcn_s_sleep(2);
        asm volatile("" ::: "memory");
        const u32 q = (u32)(bm >> (lane & 56));
        u32x4 t4[2];
#pragma unroll
        for (int t = 0; t < 2; ++t) t4[t] = ld_sc1(rs, ((q >> (4 * t)) & 15) ? off[t] : OOB);
        const bool rm = (q >> (lane & 4)) & 15;
        const u32x4 t2 = ld_sc1(rs, rm ? moff : OOB);
#pragma unroll
        for (int t = 0; t < 2; ++t) { t4[t][0] -= tag; v[t] = ((q >> (4 * t)) & 15) ? t4[t] : v[t]; }
        mv = rm ? t2 : mv;
        PIN(mv);
        asm volatile("s_nop 1");
        bm = comb(false);
        if (bm == 0ull) break;
      }
    }
    SCHED_FENCE();
    TS(8);
  }
  wg_barrier();
  SCHED_FENCE();
  TS(22);
  {
    bf16x8 lf[4];
#pragma unroll
    for (int s = 0; s < 4; ++s) lf[s] = *reinterpret_cast<const LDSP bf16x8*>((const LDSP char*)lrd + 64 * s);
    SCHED_FENCE();
    f32x4 c = f32x4{0.f, 0.f, 0.f, 0.f};
#pragma unroll
    for (int s = 0; s < 4; ++s) c = mfma16(uvf[s], lf[s], c);
    const float ya = self(m8, c[2], c[0]), yb = self(m8, c[3], c[1]);
    const float mine = self(m4, yb, ya) + dppf<0x141>(self(m4, ya, yb));
    TS(9);
    u64 t = fx_add2<4>(a.ws, rs, mine, cxa, WS_XC + (u32)((h * 4 + e4) * 512 + tid) * 16, tag, fc);
    u32x4 owf[4];
#pragma unroll
    for (int k = 0; k < 4; ++k) {
      const u32x2 f0 = __builtin_bit_cast(u32x2, kfrag(ow, 4 * (k >> 1) + (k & 1))), f1 = __builtin_bit_cast(u32x2, kfrag(ow, 4 * (k >> 1) + (k & 1) + 2));
      owf[k] = u32x4{f0[0], f0[1], f1[0], f1[1]}; PIN(owf[k]);
    }
    SCHED_FENCE();
    float x = fx_dec(t, fc.dn);
    if (__builtin_expect(!__all(__builtin_fabsf(x) < fc.rg), 0)) {
#pragma unroll 1
      for (u32 spins = 0; spins < SPIN_CAP; ++spins) {
        if (__all((u32)(fx_raw<4>(t) >> 58) == 4u)) { x = fx_val<4, 8192>(rs, fx_raw<4>(t), WS_XC + (u32)(h * 2048 + tid) * 16, tag); break; }
        u64 z = 0; PIN(z);
        t = __hip_atomic_fetch_add(reinterpret_cast<u64*>(a.ws + cxa), z, __ATOMIC_RELAXED, __HIP_MEMORY_SCOPE_AGENT) + ((0x433ull << 52) - (4ull << 58));
        x = fx_dec(t, -0x1.4p12);
        if (__all(__builtin_fabsf(x) < fc.rg)) break;
      }
    }
    TS(12);
    const u32 xb = pk2(x, x) & mh;
    const u32 P = xb | dppu<0x141>(xb);
    const u32 p0 = P & mq[0], p1 = P & mq[1];
    SCHED_FENCE();
    u32 Q = dppu<0x128>(P);
    PIN(Q);
    const u32x4 a0 = u32x4{p0, Q & mq[0], p1, Q & mq[1]}, a1 = u32x4{Q & mq[2], P & mq[2], Q & mq[3], P & mq[3]};
    f32x4 s0 = mfma16(asbf(a0), asbf(owf[0]), f32x4{0.f, 0.f, 0.f, 0.f}), s1 = mfma16(asbf(a0), asbf(owf[1]), f32x4{0.f, 0.f, 0.f, 0.f});
    s0 = mfma16(asbf(a1), asbf(owf[2]), s0); s1 = mfma16(asbf(a1), asbf(owf[3]), s1);
    *reinterpret_cast<LDSP f32x4*>((LDSP char*)exw) = s0;
    *reinterpret_cast<LDSP f32x4*>((LDSP char*)(exw ^ 16u)) = s1;
  }
  wg_barrier();
  {
    float p[8];
#pragma unroll
    for (int k = 0; k < 8; ++k) p[k] = *reinterpret_cast<const LDSP float*>((const LDSP char*)lrx + 2048 * k);
#pragma unroll
    for (int k = 1; k < 8; ++k) p[0] += p[k];
    TS(10);
    const u64 t = fx_add2<32>(a.ws, rs, p[0], aoff, WS_XB + h * 32768 + word * 16, tag, fd);
    fx_out<32>(reinterpret_cast<const char*>(a.out), rs, t, ooff, WS_XB + word * 16, tag, fd);
  }
  TS(11);
  TS_FLUSH();
}

DI void roleA(const KArgs& a, char* smem, const int g, const int idx, const int w, const int lane, const u32x4 (&R)[4], const u32 kvo, const u32 cpf, const u64 ts0_) {
  constexpr int ROLE_ = 1;
  TS_DECL; TS(0);
  const int r = lane >> 4, c16 = lane & 15, tid = w * 64 + lane;
  const int h = 4 * (idx & 7) + ((idx >> 3) & 3), b = idx >> 5;
  const __amdgpu_buffer_rsrc_t rs = __builtin_amdgcn_make_buffer_rsrc((void*)a.ws, 0, (int)WS_END, RSRC_W3);
  u32 tag = *reinterpret_cast<const u32*>(a.ws + WS_EPOCH);
  u32x4 kv[16][4];
  u32x4 ov[16];
  const u16* wop = a.woa + (size_t)(h * DH + 64 * (c16 >> 3) + 8 * r) * DM + 64 * w + 8 * (c16 & 7);
  auto wq4 = [&](int q) { return qoffs(q, b); };
  u64 wlate[2] = {(u64)(wop + (size_t)wq4(2) * DM), (u64)(wop + (size_t)wq4(3) * DM)};
  auto woload = [&](int n) {
    if (n == 7) { PIN(wlate[0]); PIN(wlate[1]); }
    if (n < 8) ov[n] = ldsh(wop + (size_t)(wq4(n >> 2) + (n & 3)) * DM);
    else ov[n] = *reinterpret_cast<const __attribute__((address_space(1))) u32x4*>(wlate[(n >> 2) & 1] + (n & 3) * (DM * 2));
  };
  const u32 org = clk32();
  const int rot = (w >> 2) * FW2;
  auto kvissue = [&](int lo, int hi) {
#pragma unroll
    for (int n = 0; n < 64; ++n) {
      if (n < lo || n >= hi) continue;
      const int ch = n >> 2, isv = ch < C0 ? ch & 1 : ch >= C0 + ND, st = ch < C0 ? ch >> 1 : 8 - ND + (ch - C0) % ND;
      if (n >= NA0 && n < NA1) soft<WSA>(org, TA0 + n * TA1);
      asm volatile("" ::: "memory");
      kv[ch][n & 3] = ldnt(reinterpret_cast<const u16*>(reinterpret_cast<const char*>(isv ? a.vc : a.kc) + (16 * (st ^ rot) + 4 * (n & 3)) * (DH * 2) + kvo));
      SCHED_FENCE();
    }
  };
  auto upto = [](int ch) { const int y = PY + CU * (ch + 1), x = 4 * (ch + AW + 1), z = ch < 0 ? PY : (x > y ? x : y); return z < 4 * NCH ? z : 4 * NCH; };
  {
    const int m0 = 128 * b + 16 * w + 4 * r;
    const u32x2 xf = ldnt8(a.xa + (c16 & 3) * DM + m0);
    SCHED_FENCE();
    __builtin_amdgcn_s_barrier();
    SCHED_FENCE();
    kvissue(0, A0);
    SCHED_FENCE();
    asm volatile("" :: "v"(cpf));
    PIN(tag); tag = next_tag(tag);
    f32x4 ac[8];
#pragma unroll
    for (int j = 0; j < 8; ++j) ac[j] = __builtin_amdgcn_mfma_f32_16x16x16bf16_1k(__builtin_bit_cast(s16x4, xf), kfrag(R, j), f32x4{0.f, 0.f, 0.f, 0.f}, 0, 0, 0);
    const f32x4 s0 = sel4(ac[0], ac[2], ac[4], ac[6], r), s1 = sel4(ac[1], ac[3], ac[5], ac[7], r);
    float* pp = reinterpret_cast<float*>(smem + L_AX) + w * 512 + 8 * c16 + 2 * r;
#pragma unroll
    for (int k = 0; k < NB; ++k) *reinterpret_cast<u32x2*>(pp + k * DH) = u32x2{__float_as_uint(s0[k]), __float_as_uint(s1[k])};
  }
  wg_barrier();
  if (w < 2) {
    const float* pp = reinterpret_cast<const float*>(smem + L_AX) + 4 * (tid & 127);
    f32x4 t[8];
#pragma unroll
    for (int k = 0; k < 8; ++k) t[k] = *reinterpret_cast<const f32x4*>(pp + 512 * k);
    f32x4 s = ((t[0] + t[1]) + (t[2] + t[3])) + ((t[4] + t[5]) + (t[6] + t[7]));
    PIN(s);
    u32x4 x;
#pragma unroll
    for (int k = 0; k < 4; ++k) x[k] = __builtin_amdgcn_perm(__float_as_uint(s[k]), tag, 0x07060500u + k);
    st_sc1(rs, WS_QX + (u32)(((h * 4 + ((tid >> 5) & 3)) * 4 + b) * DH + 4 * (lane & 31)) * 4, x);
  }
  SCHED_FENCE();
  TS(21);
  __builtin_amdgcn_s_barrier();
  SCHED_FENCE();
  kvissue(A0, PX);
  SCHED_FENCE();
  u32x4 qpk;
  {
    const u32 off = WS_QX + (u32)(((h * 4 + b) * 4 + r) * DH + 64 * (w & 1) + 4 * c16) * 4;
    u32x4 v = u32x4{0u, 0u, 0u, 0u};
    if ((w >> 1) == 1) v = ld_sc1(rs, off);
    SCHED_FENCE();
    kvissue(PX, PY);
    SCHED_FENCE();
    if ((w >> 1) == 1) q_finish(rs, off, v, tag, smem + L_AQ + 128 * (w & 1), lane);
    wg_barrier();
    qpk = *reinterpret_cast<const u32x4*>(smem + L_AQ + 16 * c16);
  }
  TS(1);
  SCHED_FENCE();
  float m = -INFINITY, l = 0.f, sd[ND];
  f32x4 acc = f32x4{0.f, 0.f, 0.f, 0.f};
  u32 pm[4], pb[ND];
  u32x4 pa[4];
#pragma unroll
  for (int k = 0; k < 4; ++k) { pm[k] = (c16 >> 1) == k ? ((c16 & 1) ? 0xffff0000u : 0xffffu) : 0u; PIN(pm[k]); }
  auto pbuild = [&](u32 x) {
    u32 t[4] = {dppu<0x150>(x), dppu<0x154>(x), dppu<0x158>(x), dppu<0x15c>(x)};
#pragma unroll
    for (int n = 0; n < 4; ++n) { PIN(t[n]); pa[n] = u32x4{t[n] & pm[0], t[n] & pm[1], t[n] & pm[2], t[n] & pm[3]}; PIN(pa[n]); }
  };
  u32x4 wf[2][8];
  auto wpack = [&](int q) {
#pragma unroll
    for (int j = 0; j < 8; ++j) {
      const u32x2 t = __builtin_bit_cast(u32x2, kfrag(ov + 4 * q, j));
      wf[q >> 1][j][2 * (q & 1)] = t[0]; wf[q >> 1][j][2 * (q & 1) + 1] = t[1];
      PIN(wf[q >> 1][j]);
    }
  };
  const bool hi8 = c16 & 8, hi4 = c16 & 4;
  u32 aaf = L_AST + (c16 >> 1) * 288 + (64 * (c16 & 1) + 8 * r) * 2, aml = L_AST + 2304 + 8 * (lane & 7), m8 = hi8 ? ~0u : 0u;
  u32 asw = r < 2 ? L_AST + w * 288 + (64 * (c16 >> 3) + 8 * (c16 & 3) + qoffs(((c16 >> 1) & 2) + r, 4 - b)) * 2 : L_AX + tid * 8;
  PIN(aaf); PIN(m8);
  const FxC fc = fx_const();
  u32 aoff = WS_ACC + b * 16384 + (tid >> 3) * 256 + (tid & 7) * 8, ooff = (b * 1024 + 64 * w + 8 * (c16 & 7) + 2 * r + (c16 >> 3)) * 2;
  PIN(aml); PIN(asw); PIN(aoff); PIN(ooff);
  const u32 foff = WS_FLG + (u32)(idx ^ (lane & 8)) * 128;
  u32x2 fl = u32x2{tag, tag};
  auto wit = [](int n) { return n < 8 ? n : n < 12 ? WL1 : WL2; };
  auto wdn = [&](int n) { return upto(n) / 4; };
  auto steps = [&](const int lo, const int hi) __attribute__((always_inline)) {
#pragma unroll
  for (int ch = 0; ch < NCH; ++ch) {
    if (ch < lo || ch >= hi) continue;
    SCHED_FENCE();
    if (ch == 12) { TS(6); SCHED_FENCE(); }
    if (ch == 11) { TS(7); SCHED_FENCE(); }
    kvissue(upto(ch - 1), upto(ch));
#pragma unroll
    for (int n = 0; n < 16; ++n)
      if (wit(n) == ch) woload(n);
    if (ch == NCH - 1) { SCHED_FENCE(); TS(17); SCHED_FENCE(); }
    if (ch == 12) { SCHED_FENCE(); TS(18); SCHED_FENCE(); }
    if (ch == 11) { SCHED_FENCE(); TS(8); SCHED_FENCE(); }
    if (ch == FLI && w == FLW) fl = __builtin_amdgcn_raw_buffer_load_b64(rs, foff, 0, 16);
    SCHED_FENCE();
    if (ASYNC > 0 && ch % ASYNC == ASYNC - 1 && ch < C0 + ND - 1) { __builtin_amdgcn_s_barrier(); SCHED_FENCE(); }
    if (ch == 3) TS(19); if (ch == 5) TS(20); if (ch == 7) TS(22); if (ch == 9) TS(10);
    if (ch < C0 && !(ch & 1)) {
      const float s = score4(kv[ch], qpk, hi8, hi4);
      if (__builtin_expect(__any(s > m), ch < 2)) {
        float mn = fmaxf(fmaxf(s, dppf<0x128>(s)), m);
        mn = x32_max(x16_max(fmaxf(mn, dppf<0x124>(mn))));
        const float alpha = __builtin_amdgcn_exp2f(m - mn);
        m = mn;
        l *= alpha;
        acc *= alpha;
      }
      const float p = __builtin_amdgcn_exp2f(s - m);
      l += p;
      pbuild(pk2(p, p));
      PIN(l);
    } else if (ch >= C0 && ch < C0 + ND - 1) {
      sd[ch - C0] = score4(kv[ch], qpk, hi8, hi4);
      PIN(sd[ch - C0]);
    } else if (ch == C0 + ND - 1) {
      sd[ND - 1] = score4(kv[ch], qpk, hi8, hi4);
      float mn = sd[0];
#pragma unroll
      for (int k = 1; k < ND; ++k) mn = fmaxf(mn, sd[k]);
      mn = fmaxf(fmaxf(mn, dppf<0x128>(mn)), m);
      mn = x32_max(x16_max(fmaxf(mn, dppf<0x124>(mn))));
      const float beta = __builtin_amdgcn_exp2f(m - mn);
      l *= beta;
#pragma unroll
      for (int k = 0; k < ND; ++k) { sd[k] = __builtin_amdgcn_exp2f(sd[k] - mn); l += sd[k]; }
      l += dppf<0x128>(l); l += dppf<0x124>(l);
      l = x32_sum(x16_sum(l));
      if (lane == 0) *reinterpret_cast<f32x2_*>(smem + L_AST + 2304 + 8 * w) = f32x2_{mn, l};
      TS(15);
      wg_barrier();
      const f32x2_ ml = *reinterpret_cast<const LDSP f32x2_*>((const LDSP char*)aml);
      float M = fmaxf(ml[0], dppf<0xB1>(ml[0]));
      M = fmaxf(M, dppf<0x4E>(M)); M = fmaxf(M, dppf<0x141>(M));
      float L = __builtin_amdgcn_exp2f(ml[0] - M) * ml[1];
      L += dppf<0xB1>(L); L += dppf<0x4E>(L); L += dppf<0x141>(L);
      const float al = __builtin_amdgcn_exp2f(mn - M) * __builtin_amdgcn_rcpf(L), ab = al * beta;
      acc *= ab; PIN(acc);
#pragma unroll
      for (int k = 0; k < ND; ++k) { pb[k] = pk2(sd[k] * al, sd[k] * al); PIN(pb[k]); }
      pbuild(pb[0]);
      TS(14);
    } else {
#pragma unroll
      for (int n = 0; n < 4; ++n) acc = mfma16(asbf(pa[n]), asbf(kv[ch][n]), acc);
      PIN(acc);
      SCHED_FENCE();
      if (ch >= C0 + ND && ch < NCH - 1) pbuild(pb[ch + 1 - C0 - ND]);
    }
#pragma unroll
    for (int q = 0; q < 2; ++q)
      if (wdn(4 * q + 3) - 1 == ch) wpack(q);
  }
  };
  steps(0, NCH - 1);
  if (__builtin_expect(!__all((fl[0] == tag) & (fl[1] == tag)), 0)) {
#pragma unroll 1
    for (u32 spins = 0; spins < SPIN_CAP; ++spins) {
      asm volatile("" ::: "memory");
      fl = __builtin_amdgcn_raw_buffer_load_b64(rs, foff, 0, 16);
      if (__all((fl[0] == tag) & (fl[1] == tag))) break;
      __builtin_amdgcn_s_sleep(2);
    }
  }
  steps(NCH - 1, NCH);
  TS(2);
#pragma unroll
  for (int n = 0; n < 16; ++n)
    if (wit(n) == NCH) woload(n);
  SCHED_FENCE();
  *reinterpret_cast<LDSP u32x2*>((LDSP char*)asw) = u32x2{pk2(acc[0], acc[1]), pk2(acc[2], acc[3])};
  SCHED_FENCE();
  wg_barrier();
  TS(5);
  {
    const bf16x8 af0 = *reinterpret_cast<const LDSP bf16x8*>((const LDSP char*)aaf), af1 = *reinterpret_cast<const LDSP bf16x8*>((const LDSP char*)aaf + 64);
    SCHED_FENCE();
    f32x4 D[8];
    float u[4], v[2];
    auto JO = [](int j) { return (j & 1) | ((j & 2) << 1) | ((j & 4) >> 1); };
    auto last = [&](int j) { const u32x2 t = __builtin_bit_cast(u32x2, kfrag(ov + 12, j)); wf[1][j][2] = t[0]; wf[1][j][3] = t[1]; PIN(wf[1][j]); };
    auto pair = [&](int k) {
      const f32x4 e = D[2 * k], o = D[2 * k + 1];
      const float sn = self(m8, e[1], o[0]) + self(m8, e[3], o[2]);
      SCHED_FENCE();
      u[k] = (self(m8, o[1], e[0]) + self(m8, o[3], e[2])) + dppf<0x128>(sn);
      SCHED_FENCE();
    };
    auto quad = [&](int k) { auto x = __builtin_amdgcn_permlane32_swap(__float_as_uint(u[k]), __float_as_uint(u[k + 2]), false, false); v[k] = __uint_as_float(x[0]) + __uint_as_float(x[1]); SCHED_FENCE(); };
#pragma unroll
    for (int j = 0; j < 8; ++j) {
      D[JO(j)] = mfma16(af0, asbf(wf[0][JO(j)]), f32x4{0.f, 0.f, 0.f, 0.f});
      SCHED_FENCE();
      wf[1][JO(j)] = __builtin_bit_cast(u32x2, kfrag(ov + 8, JO(j))).xyxy;
      PIN(wf[1][JO(j)][0]); PIN(wf[1][JO(j)][1]);
      SCHED_FENCE();
    }
    last(JO(0));
#pragma unroll
    for (int j = 0; j < 8; ++j) {
      if (j < 7) last(JO(j + 1));
      SCHED_FENCE();
      D[JO(j)] = mfma16(af1, asbf(wf[1][JO(j)]), D[JO(j)]);
      SCHED_FENCE();
      if (j == 4) pair(0);
      if (j == 6) pair(2);
      if (j == 7) {
        const float sn = self(m8, D[2][1], D[3][0]) + self(m8, D[2][3], D[3][2]);
        SCHED_FENCE();
        const float kp = self(m8, D[3][1], D[2][0]) + self(m8, D[3][3], D[2][2]);
        SCHED_FENCE();
        quad(0);
        u[1] = kp + dppf<0x128>(sn);
        SCHED_FENCE();
        const float k1 = self(m8, D[7][1], D[6][0]);
        SCHED_FENCE();
        const float s3 = self(m8, D[6][1], D[7][0]) + self(m8, D[6][3], D[7][2]);
        SCHED_FENCE();
        const float k2 = self(m8, D[7][3], D[6][2]);
        SCHED_FENCE();
        u[3] = (k1 + dppf<0x128>(s3)) + k2;
        SCHED_FENCE();
        quad(1);
      }
      SCHED_FENCE();
    }
    const auto x = __builtin_amdgcn_permlane16_swap(__float_as_uint(v[0]), __float_as_uint(v[1]), false, false);
    const float yy = __uint_as_float(x[0]) + __uint_as_float(x[1]);
    TS(3);
    u64 t = fx_add2<32>(a.ws, rs, yy, aoff, WS_XA + h * 32768 + (b * 512 + tid) * 16, tag, fc);
    fx_out<32>(reinterpret_cast<const char*>(a.out), rs, t, ooff, WS_XA + (b * 512 + tid) * 16, tag, fc);
  }
  TS(11);
  TS_FLUSH();
}

__global__ void __launch_bounds__(512, 2) fused(KArgs a) {
  const u64 ts0_ = __builtin_amdgcn_s_memrealtime(); SCHED_FENCE();
  extern __shared__ __attribute__((aligned(16))) char smem[];
  const int tid = threadIdx.x, lane = tid & 63, w = __builtin_amdgcn_readfirstlane(tid >> 6), g = blockIdx.x;
  const int idx = (g & 7) | ((g >> 4) << 3);
  u32 cpf = 0;
  if (w >= 8 - CPF && (idx >> 3) < CPN) {
    u64 pc; asm volatile("s_getpc_b64 %0" : "=s"(pc));
    cpf = *reinterpret_cast<const __attribute__((address_space(1))) u32*>((pc & ~127ull) + min((7 - w) * 64 + lane, CPL) * 128);
  }
  SCHED_FENCE();
  u32 o[4], o2;
  {
    const u32 h = 4 * (idx & 7) + ((idx >> 3) & 3), b = idx >> 5, r = lane >> 4;
#pragma unroll
    for (int n = 0; n < 4; ++n) { o[n] = (128 * b + 16 * w + 4 * r + n) * (NH * DH * 2) + h * (DH * 2) + (lane & 15) * 16; PIN(o[n]); }
    u32 ob = ((((idx & 7) * 16 + (idx >> 3)) * 128 + w) * DC + 8 * lane) * 2, oa = (((b * NH + h) * SK + (SK - WIN) + 128 * w + r) * DH + 8 * (lane & 15)) * 2;
    PIN(oa); PIN(ob);
    o2 = ((g >> 3) & 1) ? ob : oa;
    PIN(o2);
  }
  SCHED_FENCE();
  asm volatile("" :: "s"(a.xa), "s"(a.kc), "s"(a.vc), "s"(a.wqa), "s"(a.woa), "s"(a.xb), "s"(a.ckv), "s"(a.wqb), "s"(a.wuk), "s"(a.wuv), "s"(a.wob), "s"(a.ws), "s"(a.out));
  u32x4 R[4];
  {
    const char* p = reinterpret_cast<const char*>(((g >> 3) & 1) ? a.wuk : a.wqa);
#pragma unroll
    for (int n = 0; n < 4; ++n) R[n] = ldnt(reinterpret_cast<const u16*>(p + o[n]));
    SCHED_FENCE();
  }
  int g2 = g, l2 = lane;
  if (__builtin_expect((g >> 3) & 1, 0)) {
    roleB(a, smem, g, idx, w, lane, R, o2, cpf, ts0_);
    asm volatile("" : "=v"(R[0]), "=v"(R[1]), "=v"(R[2]), "=v"(R[3]), "=v"(o2), "=v"(cpf), "=v"(l2));
  }
  asm volatile("" : "+s"(g2));
  if (__builtin_expect(!((g2 >> 3) & 1), 1)) roleA(a, smem, g, idx, w, l2 & 63, R, o2, cpf, ts0_);
}

extern "C" void kernel_launch(void* const* d_in, const int* in_sizes, int n_in,
                              void* d_out, int out_size, void* d_ws, size_t ws_size,
                              hipStream_t stream) {
  KArgs a;
  a.xa  = (const u16*)d_in[0];
  a.kc  = (const u16*)d_in[1];
  a.vc  = (const u16*)d_in[2];
  a.wqa = (const u16*)d_in[3];
  a.woa = (const u16*)d_in[4];
  a.xb  = (const u16*)d_in[5];
  a.ckv = (const u16*)d_in[6];
  a.wqb = (const u16*)d_in[7];
  a.wuk = (const u16*)d_in[8];
  a.wuv = (const u16*)d_in[9];
  a.wob = (const u16*)d_in[10];
  {
    const size_t need = (size_t)WS_END + (1u << 20);
    const size_t off = ws_size > need ? (((ws_size - need) / 8 * WOFF) & ~(size_t)0x1fffff) : 0;
    a.ws = (char*)d_ws + off;
  }
  a.out = (u16*)d_out;
  (void)hipFuncSetAttribute((const void*)fused, hipFuncAttributeMaxDynamicSharedMemorySize, L_END);
  fused<<<256, 512, L_END, stream>>>(a);
}
```

```cpp
#pragma clang diagnostic ignored "-Wint-to-pointer-cast"
#include <hip/hip_runtime.h>
#include <hip/hip_bf16.h>
#include <stdint.h>

typedef unsigned int u32;
typedef unsigned short u16;
typedef unsigned long long u64;
using u32x4  = __attribute__((ext_vector_type(4))) unsigned;
using u32x2  = __attribute__((ext_vector_type(2))) unsigned;
using f32x4  = __attribute__((ext_vector_type(4))) float;
using bf16x8 = __attribute__((ext_vector_type(8))) short;
using s16x4  = __attribute__((ext_vector_type(4))) short;
#define DI __device__ __forceinline__
#define LDSP __attribute__((address_space(3)))
#define SCHED_FENCE() __builtin_amdgcn_sched_barrier(0)
#define PIN(x) asm volatile("" : "+v"(x))

using f32x2_ = __attribute__((ext_vector_type(2))) float;
using bf16x2_ = __attribute__((ext_vector_type(2))) __bf16;
DI u32 pk2(float lo, float hi) { return __builtin_bit_cast(u32, __builtin_convertvector(f32x2_{lo, hi}, bf16x2_)); }
template <int CTRL> DI float dppf(float v) { return __builtin_bit_cast(float, __builtin_amdgcn_update_dpp(0, __builtin_bit_cast(int, v), CTRL, 0xf, 0xf, true)); }
template <int CTRL> DI u32 dppu(u32 v) { return (u32)__builtin_amdgcn_update_dpp(0, (int)v, CTRL, 0xf, 0xf, true); }
DI float row16_maxq(float v) {
  asm volatile("s_nop 1\n\tv_max_f32_dpp %0, %0, %0 row_ror:8 row_mask:0xf bank_mask:0xf\n\ts_nop 1\n\tv_max_f32_dpp %0, %0, %0 row_ror:4 row_mask:0xf bank_mask:0xf\n\t"
               "s_nop 1\n\tv_max_f32_dpp %0, %0, %0 row_ror:2 row_mask:0xf bank_mask:0xf\n\ts_nop 1\n\tv_max_f32_dpp %0, %0, %0 row_ror:1 row_mask:0xf bank_mask:0xf\n\ts_nop 1" : "+v"(v));
  return v;
}
DI float x16_sum(float v) { auto s = __builtin_amdgcn_permlane16_swap(__float_as_uint(v), __float_as_uint(v), false, false); return __uint_as_float(s[0]) + __uint_as_float(s[1]); }
DI float x32_sum(float v) { auto s = __builtin_amdgcn_permlane32_swap(__float_as_uint(v), __float_as_uint(v), false, false); return __uint_as_float(s[0]) + __uint_as_float(s[1]); }
DI float x16_max(float v) { auto s = __builtin_amdgcn_permlane16_swap(__float_as_uint(v), __float_as_uint(v), false, false); return fmaxf(__uint_as_float(s[0]), __uint_as_float(s[1])); }
DI float x32_max(float v) { auto s = __builtin_amdgcn_permlane32_swap(__float_as_uint(v), __float_as_uint(v), false, false); return fmaxf(__uint_as_float(s[0]), __uint_as_float(s[1])); }
using bf16x2 = __attribute__((ext_vector_type(2))) __bf16;
DI float dot2(u32 a, u32 b, float c) { return __builtin_amdgcn_fdot2_f32_bf16(__builtin_bit_cast(bf16x2, a), __builtin_bit_cast(bf16x2, b), c, false); }
DI u32 plo(u32 r1, u32 r0) { return __builtin_amdgcn_perm(r1, r0, 0x05040100u); }
DI u32 phi(u32 r1, u32 r0) { return __builtin_amdgcn_perm(r1, r0, 0x07060302u); }
DI bf16x8 bfrag(const u32x4* R, int j) {
  const int q = j >> 1;
  u32x4 o;
  if (j & 1) { o[0] = phi(R[1][q], R[0][q]); o[1] = phi(R[3][q], R[2][q]); o[2] = phi(R[5][q], R[4][q]); o[3] = phi(R[7][q], R[6][q]); }
  else       { o[0] = plo(R[1][q], R[0][q]); o[1] = plo(R[3][q], R[2][q]); o[2] = plo(R[5][q], R[4][q]); o[3] = plo(R[7][q], R[6][q]); }
  return __builtin_bit_cast(bf16x8, o);
}
DI s16x4 kfrag(const u32x4* R, int j) {
  const int q = j >> 1;
  u32x2 o;
  if (j & 1) { o[0] = phi(R[1][q], R[0][q]); o[1] = phi(R[3][q], R[2][q]); }
  else       { o[0] = plo(R[1][q], R[0][q]); o[1] = plo(R[3][q], R[2][q]); }
  return __builtin_bit_cast(s16x4, o);
}
DI u32 selm(u32 m, u32 x, u32 y) { return __builtin_amdgcn_bitop3_b32(m, x, y, 0xca); }
DI float self(u32 m, float x, float y) { return __uint_as_float(selm(m, __float_as_uint(x), __float_as_uint(y))); }
DI f32x4 sel4(f32x4 a, f32x4 b, f32x4 c, f32x4 d, int r) { f32x4 lo = (r & 1) ? b : a, hi = (r & 1) ? d : c; return (r & 2) ? hi : lo; }
DI f32x4 mfma16(bf16x8 a, bf16x8 b, f32x4 c) { return __builtin_amdgcn_mfma_f32_16x16x32_bf16(a, b, c, 0, 0, 0); }
DI bf16x8 asbf(u32x4 v) { return __builtin_bit_cast(bf16x8, v); }
DI float dot8(const u32x4 kv, const u32x4 q) {
  float s = dot2(kv[0], q[0], 0.f);
  s = dot2(kv[1], q[1], s); s = dot2(kv[2], q[2], s); s = dot2(kv[3], q[3], s);
  return s;
}
constexpr int NB = 4, NH = 32, DH = 128, SK = 4096, WIN = 1024, DM = 512, DC = 512;
constexpr float LOG2E = 1.4426950408889634f;
constexpr float SCL2 = 0.088388347648318440f * LOG2E;

constexpr int WOFF = 8;
constexpr int AW = 3;
constexpr int ASYNC = 2;
constexpr int A0 = 4;
constexpr int PX = 11;
constexpr int PY = 16;
constexpr int CU = 2;
constexpr int ND = 3;
constexpr int NA0 = 4;
constexpr int NA1 = 26;
constexpr int TA0 = 500;
constexpr int TA1 = 235;
constexpr int WSA = 2;
constexpr int NB0 = 3;
constexpr int WSB = 2;
constexpr int N3 = 20;
constexpr int SP0 = 2;
constexpr int FLI = 10;
constexpr int FLW = 0;
constexpr int CPF = 3;
constexpr int CPN = 1;
constexpr int CPL = 178;
constexpr int CPL2 = 40;
constexpr int FW2 = 3;
constexpr int WL1 = 15;
constexpr int WL2 = 16;
constexpr int THC = 275;

constexpr int C0 = 16 - 2 * ND;
constexpr int NCH = 16;
constexpr u32 WS_EPOCH = 0;
constexpr u32 WS_QP    = 4096;
constexpr u32 PBLK     = 4608;
constexpr u32 WS_PART  = WS_QP + NB * 8 * 384 * 16;
constexpr u32 WS_QX    = WS_PART + NB * 16 * 8 * PBLK;
constexpr u32 WS_ACC   = WS_QX + NH * NB * 4 * DH * 4;
constexpr u32 WS_ACX   = WS_ACC + 2 * 65536;
constexpr u32 WS_FLG   = WS_ACX + 32 * 4096;
constexpr u32 WS_XA    = WS_FLG + 128 * 128;
constexpr u32 WS_XB    = WS_XA + 64 * 2048 * 16;
constexpr u32 WS_XC    = WS_XB + 32 * 2048 * 16;
constexpr u32 WS_END   = WS_XC + 32 * 2048 * 16;
constexpr u32 RSRC_W3  = 0x00020000;
constexpr u32 SPIN_CAP = 1u << 17;

constexpr int L_T    = 0;
constexpr int L_Q    = 131072;
constexpr int L_P    = L_Q + 16384;
constexpr int L_MX   = L_P + 8192;
constexpr int L_SM   = L_MX + 512;
constexpr int L_S1P  = 12288;
constexpr int L_UKT  = 8192;
constexpr int L_QB   = L_Q;
constexpr int L_QO   = L_Q + 1024;
constexpr int L_LAT  = L_P;
constexpr int L_EX   = L_Q;
constexpr int L_ENDB = 163840;
constexpr int L_AQ   = 4096;
constexpr int L_AST  = L_AQ + 2048;
constexpr int L_AX   = L_AST + 20736;
constexpr int L_END  = L_ENDB;

DI u32 fsw(u32 key) { return (((key >> 1) & 1) << 2) | (((key >> 3) & 1) << 1); }
struct KArgs {
  const u16* xa; const u16* kc; const u16* vc; const u16* wqa; const u16* woa;
  const u16* xb; const u16* ckv; const u16* wqb; const u16* wuk; const u16* wuv; const u16* wob;
  char* ws; u16* out;
};

DI u32x4 ldnt(const u16* p) { return __builtin_nontemporal_load(reinterpret_cast<const u32x4*>(p)); }
DI u32x2 ldnt8(const u16* p) { return __builtin_nontemporal_load(reinterpret_cast<const u32x2*>(p)); }
DI u32x4 ldsh(const u16* p) { return *reinterpret_cast<const u32x4*>(p); }
DI u32x4 ld_sc1(__amdgpu_buffer_rsrc_t rs, u32 off) { return __builtin_amdgcn_raw_buffer_load_b128(rs, off, 0, 16); }
DI u32x2 ld_sc1_64(__amdgpu_buffer_rsrc_t rs, u32 off) { return __builtin_amdgcn_raw_buffer_load_b64(rs, off, 0, 16); }
DI void st_sc1(__amdgpu_buffer_rsrc_t rs, u32 off, u32x4 v) { __builtin_amdgcn_raw_buffer_store_b128(v, rs, off, 0, 16); }
DI void st_sc1_32(__amdgpu_buffer_rsrc_t rs, u32 off, u32 v) { __builtin_amdgcn_raw_buffer_store_b32(v, rs, off, 0, 16); }
DI void wg_barrier() { asm volatile("s_waitcnt lgkmcnt(0)\n\ts_barrier" ::: "memory"); }

template <int NARR, int XSTR>
DI float fx_val(__amdgpu_buffer_rsrc_t rs, u64 t, u32 x0, u32 tag) {
  float s = (float)__builtin_fma(__builtin_bit_cast(double, (t & 0x7ffffffffffffull) | 0x4330000000000000ull), 0x1p-40, -0x1.4p12);
  const u32 np = (u32)(t >> 51) & 127u;
  if (__builtin_expect(np != 0u, 0)) {
    float e = 0.f;
#pragma unroll 1
    for (u32 spins = 0; spins < 64; ++spins) {
      u32 c = 0; e = 0.f;
#pragma unroll 4
      for (u32 p = 0; p < NARR; ++p) {
        const u32x4 v = ld_sc1(rs, x0 + p * XSTR);
        const bool ok = (v[1] == tag) & (v[3] == tag) & (v[0] == ~v[2]);
        e += ok ? __uint_as_float(v[0]) : 0.f; c += ok;
      }
      if (c == np) break;
    }
    s += e;
  }
  return s;
}
struct FxC { double up, dn; float hi, lo, rg; };
DI FxC fx_const() { FxC c = {0x1.8p52, -0x1.4p12, 16.f, 0x1p-24f, 1024.f}; PIN(c.up); PIN(c.dn); asm volatile("" : "+s"(c.hi), "+s"(c.lo), "+s"(c.rg)); return c; }
template <int NARR> DI u64 fx_add2(char* ws, __amdgpu_buffer_rsrc_t rs, float y, u32 aoff, u32 xown, u32 tag, const FxC& c) {
  const float t = __builtin_fabsf(y);
  u64 x = __builtin_bit_cast(u64, __builtin_fma((double)y, 0x1p40, c.up)) + (0x0400000000000000ull - 0x4338000000000000ull);
  PIN(x);
  const u64 big = __ballot(!(t < c.hi)), some = __ballot(t >= c.lo);
  u64 odd;
  asm("s_cmp_eq_u64 %2, 0\n\ts_cselect_b64 %0, -1, %1" : "=s"(odd) : "s"(big), "s"(some) : "scc");
  if (__builtin_expect(odd != 0ull, 0)) {
    if (__ballot(t != 0.f) != 0ull) {
      st_sc1(rs, xown, u32x4{__float_as_uint(y), tag, ~__float_as_uint(y), tag});
      x = 0x0408000000000000ull;
      asm volatile("s_waitcnt vmcnt(0)" ::: "memory");
    }
  }
  const u64 o = __hip_atomic_fetch_add(reinterpret_cast<u64*>(ws + aoff), x, __ATOMIC_RELAXED, __HIP_MEMORY_SCOPE_AGENT);
  SCHED_FENCE();
  x += (0x433ull << 52) - ((u64)NARR << 58);
  PIN(x);
  return o + x;
}
DI float fx_dec(u64 d, double dn) { return (float)__builtin_fma(__builtin_bit_cast(double, d), 0x1p-40, dn); }
template <int NARR> DI u64 fx_raw(u64 d) { return d - ((0x433ull << 52) - ((u64)NARR << 58)); }
template <int NARR> DI void fx_out(const char* out, __amdgpu_buffer_rsrc_t rs, u64 d, u32 ooff, u32 x0, u32 tag, const FxC& c) {
  float s = fx_dec(d, c.dn);
  if (__builtin_fabsf(s) < c.rg) *reinterpret_cast<u16*>(const_cast<char*>(out) + ooff) = (u16)pk2(s, s);
  SCHED_FENCE();
  PIN(s);
  const u64 t = fx_raw<NARR>(d);
  if (__builtin_expect(!(__builtin_fabsf(s) < c.rg) & ((u32)(t >> 58) == (NARR & 63)), 0)) { const float e = fx_val<NARR, 32768>(rs, t, x0, tag); *reinterpret_cast<u16*>(const_cast<char*>(out) + ooff) = (u16)pk2(e, e); }
}
template <int CTRL, int BANK> DI u32 dppb(u32 old, u32 v) { return (u32)__builtin_amdgcn_update_dpp((int)old, (int)v, CTRL, 0xf, BANK, false); }
constexpr u32 OOB = 0x80000000u;
template <int N>
DI void row_issue(__amdgpu_buffer_rsrc_t rs, const u32 (&off)[N], u32x4 (&v)[N]) {
#pragma unroll
  for (int p = 0; p < N; ++p) v[p] = ld_sc1(rs, off[p]);
}
template <int N>
DI void row_wait(__amdgpu_buffer_rsrc_t rs, const u32 (&off)[N], u32x4 (&v)[N], u32 tag, bool act, int lane) {
  bool ok = true;
#pragma unroll
  for (int p = 0; p < N; ++p) PIN(v[p]);
#pragma unroll
  for (int p = 0; p < N; ++p) ok &= (v[p][3] == tag);
  if (__builtin_expect(!__all(ok | !act), 0)) {
#pragma unroll 1
    for (u32 spins = 0; spins < SPIN_CAP; ++spins) {
      __builtin_amdgcn_s_sleep(2);
      asm volatile("" ::: "memory");
      u32x4 t[N]; bool re[N];
#pragma unroll
      for (int p = 0; p < N; ++p) {
        const u64 m = __ballot(act & !((v[p][3] == tag)));
        re[p] = ((u32)(m >> (lane & 48)) & 0xffffu) != 0u;
        t[p] = ld_sc1(rs, re[p] ? off[p] : OOB);
      }
      bool k2 = true;
#pragma unroll
      for (int p = 0; p < N; ++p) { v[p] = re[p] ? t[p] : v[p]; PIN(v[p]); k2 &= (v[p][3] == tag); }
      if (__all(k2 | !act)) break;
    }
  }
}
template <int W> DI void window() { asm volatile("s_waitcnt vmcnt(%0)" :: "n"(W - 1) : "memory"); SCHED_FENCE(); }
template <int W> DI void soft(u32 org, int ns) {
  u64 t; u32 o;
  asm volatile("s_memrealtime %0\n\ts_waitcnt lgkmcnt(0)" : "=s"(t) :: "memory");
  asm volatile("s_sub_i32 %0, %1, %4\n\ts_cmp_gt_i32 %0, %2\n\ts_cbranch_scc1 1f\n\ts_waitcnt vmcnt(%3)\n1:" : "=&s"(o) : "s"((u32)t), "n"(ns / 10), "n"(W - 1), "s"(org) : "memory", "scc");
  SCHED_FENCE();
}
DI u32 clk32() { return (u32)__builtin_amdgcn_s_memrealtime(); }
DI void hint_wait(__amdgpu_buffer_rsrc_t rs, u32 off, u32x2 hv, u32 tag, int dt) {
  PIN(hv);
  if (__builtin_expect(!__all(hv[1] == tag), 0)) {
#pragma unroll 1
    for (u32 spins = 0; spins < 8; ++spins) {
      asm volatile("" ::: "memory");
      const u32x2 t = ld_sc1_64(rs, hv[1] != tag ? off : OOB);
      hv = hv[1] != tag ? t : hv;
      PIN(hv);
      if (__all(hv[1] == tag)) break;
    }
  }
  const u32 t0 = clk32();
  const float d = row16_maxq(hv[1] == tag ? (float)(int)(hv[0] - t0) : -1000.f);
  const int n = min(__builtin_amdgcn_readfirstlane((int)d) + dt, 250);
#pragma unroll 1
  for (int i = 0; i < 256 && (int)(clk32() - t0) < n; ++i) __builtin_amdgcn_s_sleep(1);
  asm volatile("" ::: "memory");
}
DI u32 next_tag(u32 e) { return e * 1664525u + 1013904223u; }

DI void s1_mfma(const u32x4 (&R)[2][8], const u32x4 (&xf)[2], int r, f32x4& s0, f32x4& s1) {
  f32x4 acc[8];
#pragma unroll
  for (int j = 0; j < 8; ++j) acc[j] = mfma16(asbf(xf[0]), bfrag(R[0], j), f32x4{0.f, 0.f, 0.f, 0.f});
#pragma unroll
  for (int j = 0; j < 8; ++j) PIN(acc[j]);
  SCHED_FENCE();
#pragma unroll
  for (int j = 0; j < 8; ++j) acc[j] = mfma16(asbf(xf[1]), bfrag(R[1], j), acc[j]);
  s0 = sel4(acc[0], acc[2], acc[4], acc[6], r); s1 = sel4(acc[1], acc[3], acc[5], acc[7], r);
}
DI int qoffs(int qd, int rot) { const int aq = (qd + rot) & 3; return 32 * (aq >> 1) + 4 * (aq & 1); }
template <class F> DI void s1_load(const u16* wsrc, const u16* xsrc, int rot, u32x4 (&R)[2][8], u32x4 (&xf)[2], F mid) {
#pragma unroll
  for (int k = 0; k < 2; ++k) {
    const u32x2 lo = ldnt8(xsrc + qoffs(2 * k, rot)), hi = ldnt8(xsrc + qoffs(2 * k + 1, rot));
    xf[k] = u32x4{lo[0], lo[1], hi[0], hi[1]};
  }
#pragma unroll
  for (int qd = 0; qd < 4; ++qd)
#pragma unroll
    for (int i = 0; i < 4; ++i) {
      if (i == 0 && qd == 2) mid();
      if (i == 0 && qd > 0) { SCHED_FENCE(); __builtin_amdgcn_s_barrier(); SCHED_FENCE(); }
      if (qd == 0) window<WSB>();
      R[qd >> 1][4 * (qd & 1) + i] = ldsh(wsrc + (size_t)(qoffs(qd, rot) + i) * (NH * DH));
    }
}

DI float score4(const u32x4 (&k)[4], const u32x4& qpk, bool hi8, bool hi4) {
  const float d0 = dot8(k[0], qpk), d1 = dot8(k[1], qpk), d2 = dot8(k[2], qpk), d3 = dot8(k[3], qpk);
  const float a0 = (hi8 ? d2 : d0) + dppf<0x128>(hi8 ? d0 : d2), a1 = (hi8 ? d3 : d1) + dppf<0x128>(hi8 ? d1 : d3);
  float s = (hi4 ? a1 : a0) + dppf<0x141>(hi4 ? a0 : a1);
  s += dppf<0xB1>(s); s += dppf<0x4E>(s);
  return s * SCL2;
}
DI u32 qtag(const u32x4& v) { return __builtin_amdgcn_perm(__builtin_amdgcn_perm(v[3], v[2], 0x04000400u), __builtin_amdgcn_perm(v[1], v[0], 0x04000400u), 0x05040100u); }
DI void q_finish(__amdgpu_buffer_rsrc_t rs, u32 off, u32x4 v, u32 tag, char* dst, int lane) {
  PIN(v);
  if (__builtin_expect(!__all(qtag(v) == tag), 0)) {
#pragma unroll 1
    for (u32 spins = 0; spins < SPIN_CAP; ++spins) {
      asm volatile("" ::: "memory");
      v = ld_sc1(rs, off);
      PIN(v);
      if (__all(qtag(v) == tag)) break;
      __builtin_amdgcn_s_sleep(1);
    }
  }
  float q[4];
#pragma unroll
  for (int k = 0; k < 4; ++k) q[k] = x32_sum(x16_sum(__uint_as_float(v[k] & ~255u)));
  if (lane < 16) reinterpret_cast<u32x2*>(dst)[lane] = u32x2{pk2(q[0], q[1]), pk2(q[2], q[3])};
}

DI void roleB(const KArgs& a, char* smem, const int g, const int idx, const int w, const int lane, const u32x4 (&uk)[4], const u32 tvo, const u32 cpf) {
  const int tid = w * 64 + lane, r = lane >> 4, c16 = lane & 15;
  const int h = 4 * (idx & 7) + ((idx >> 3) & 3), e4 = idx >> 5;
  const int b3 = (idx >> 1) & 3, hh = (idx >> 3) & 1, kp = (idx & 1) * 8 + (idx >> 4);
  const __amdgpu_buffer_rsrc_t rs = __builtin_amdgcn_make_buffer_rsrc((void*)a.ws, 0, (int)WS_END, RSRC_W3);
  u32 tag = *reinterpret_cast<const u32*>(a.ws + WS_EPOCH);
  u32x4 tv[32], uv[4], ow[4];
  const u16* owp = a.wob + (size_t)(h * DH + 16 * w) * DM + 128 * e4;
  const u16* uvp = a.wuv + ((size_t)(128 * e4 + 16 * w) * NH + h) * DH;
  u32 owo = (4 * r * DM + 8 * c16) * 2, uvo = (4 * r * NH * DH + 8 * c16) * 2;
  const u32 tso = tvo ^ (128 * DC * 2);
  auto rows = [&](int lo, int hi) {
#pragma unroll
    for (int n = 0; n < 32; ++n)
      if (n >= lo && n < hi && !(n & 8)) {
        if (n >= NB0 && n < 8) window<WSB>();
        tv[n] = ldsh(reinterpret_cast<const u16*>(reinterpret_cast<const char*>(a.ckv) + ((n >> 1 & 8) + (n & 7)) * (8 * DC * 2) + tvo));
        SCHED_FENCE();
      }
#pragma unroll
    for (int n = 0; n < 32; ++n)
      if (n >= lo && n < hi && (n & 8)) { tv[n] = ldnt(reinterpret_cast<const u16*>(reinterpret_cast<const char*>(a.ckv) + ((n >> 1 & 8) + (n & 7)) * (8 * DC * 2) + tso)); SCHED_FENCE(); }
  };
  u32 atw[2];
#pragma unroll
  for (int k = 0; k < 2; ++k) atw[k] = L_T + (lane >> 3) * 16384 + w * 128 + (((lane & 7) ^ fsw(8 * k + w)) << 4);
  auto tile = [&](int lo, int hi) {
#pragma unroll
    for (int n = 0; n < 32; ++n) if (n >= lo && n < hi) *reinterpret_cast<LDSP u32x4*>((LDSP char*)atw[n & 1] + (n & 15) * 1024) = tv[n];
    SCHED_FENCE();
  };
  u32x4 ua[4];
  {
    const int m0 = 64 * w + 8 * r;
    const u16* wsrc = a.wqb + (size_t)m0 * (NH * DH) + h * DH + 8 * c16;
    const u16* xsrc = a.xb + (c16 & 3) * DM + m0;
    u32x4 R[2][8], xf[2];
    if (w >= 4 && hh != (h >> 4)) st_sc1(rs, WS_ACC + (u32)((((idx & 7) | ((idx >> 4) << 3)) * 4 + w - 4) * 64 + lane) * 16, u32x4{0u, 0x40000u, 0u, 0x40000u});
    SCHED_FENCE();
    rows(0, 8);
    auto early = [&]() {
      SCHED_FENCE();
      asm volatile("" :: "v"(cpf));
      PIN(tag); tag = next_tag(tag);
      char* us = smem + L_UKT + w * 16384;
#pragma unroll
      for (int i = 0; i < 4; ++i) *reinterpret_cast<u32x4*>(us + (4 * r + i) * 256 + ((c16 ^ (4 * r + i)) << 4)) = uk[i];
      asm volatile("" ::: "memory");
#pragma unroll
      for (int i = 0; i < 4; ++i) ua[i] = *reinterpret_cast<const u32x4*>(us + c16 * 256 + (((4 * i + r) ^ c16) << 4));
#pragma unroll
      for (int i = 0; i < 4; ++i) PIN(ua[i]);
      SCHED_FENCE();
      PIN(atw[0]); PIN(atw[1]);
      tile(0, 8);
    };
    s1_load(wsrc, xsrc, e4, R, xf, early);
    SCHED_FENCE();
    rows(8, 16);
    f32x4 s0, s1;
    s1_mfma(R, xf, r, s0, s1);
    float* pp = reinterpret_cast<float*>(smem + L_S1P) + w * 4096 + 8 * c16 + 2 * r;
#pragma unroll
    for (int b = 0; b < NB; ++b) *reinterpret_cast<u32x2*>(pp + b * DH) = u32x2{__float_as_uint(s0[b]), __float_as_uint(s1[b])};
  }
  wg_barrier();
  SCHED_FENCE();
  rows(16, N3);
  {
    const float* pp = reinterpret_cast<const float*>(smem + L_S1P) + tid;
    const float s = ((pp[0] + pp[4096]) + (pp[8192] + pp[12288])) + ((pp[16384] + pp[20480]) + (pp[24576] + pp[28672]));
    reinterpret_cast<u16*>(smem + L_QB)[tid] = (u16)pk2(s, s);
  }
  wg_barrier();
  SCHED_FENCE();
  {
    f32x4 d0 = f32x4{0.f, 0.f, 0.f, 0.f};
#pragma unroll
    for (int i = 0; i < 4; ++i) {
      const bf16x8 qf = *reinterpret_cast<const bf16x8*>(smem + L_QB + ((c16 & 3) * DH + 32 * i + 8 * r) * 2);
      d0 = mfma16(asbf(ua[i]), qf, d0);
    }
    if (c16 < 4) {
      char* o = smem + L_QO + w * 256 + c16 * 48;
      *reinterpret_cast<u32*>(o + (r < 3 ? 16 * r : 8)) = pk2(d0[0], d0[1]);
      *reinterpret_cast<u32*>(o + (r < 3 ? 16 * r + 4 : 24)) = pk2(d0[2], d0[3]);
    }
    SCHED_FENCE();
    tile(8, 16);
    wg_barrier();
    if (w < 4 && lane < 24) {
      u32x4 x = *reinterpret_cast<const u32x4*>(smem + L_QO + (lane / 3) * 256 + (3 * w + lane % 3) * 16);
      x[3] = tag; x[2] = lane % 3 == 2 ? x[1] : x[2];
      st_sc1(rs, WS_QP + (u32)(((w * 4 + e4) * 32 + h) * 24 + lane) * 16, x);
    }
  }
  SCHED_FENCE();
  rows(N3, 24);
  constexpr int QN = 3;
  u32x4 qv[QN]; u32 qoff[QN], pa64[QN], pa32[QN];
#pragma unroll
  for (int k = 0; k < QN; ++k) { qoff[k] = WS_QP + (u32)((b3 * 8 + (w & 6) + hh) * 384 + 192 * (w & 1) + 64 * k + lane) * 16; PIN(qoff[k]); }
  u32 ack[2][2], atf[4];
  {
    const u32 rho = 32 * (w >> 1) + 8 * (c16 >> 2) + 4 * (w & 1) + (c16 & 3), tq = c16 >> 2, tp = lane & 3, rr = 8 * r + tq;
#pragma unroll
    for (int k = 0; k < 4; ++k) { ack[k >> 1][k & 1] = L_T + (k >> 1) * 65536 + rho * 128 + (((4 * (k & 1) + r) ^ fsw(rho)) << 4); PIN(ack[k >> 1][k & 1]); }
#pragma unroll
    for (int ct = 0; ct < 4; ++ct) { atf[ct] = L_T + w * 16384 + rr * 128 + (((2 * ct + (tp >> 1)) ^ fsw(rr)) << 4) + 8 * (tp & 1); PIN(atf[ct]); }
  }
  u32x4 ck[2][16];
  bf16x8 tf[2][16];
  auto ckread = [&](int t, int s) { ck[t][s] = *reinterpret_cast<const LDSP u32x4*>((const LDSP char*)ack[s >> 3][s & 1] + ((s >> 1) & 3) * 16384); };
  auto tfread = [&](int t, int lo, int hi) {
#pragma unroll
    for (int k = 0; k < 16; ++k) {
      if (k < lo || k >= hi) continue;
      s16x4 b0 = __builtin_amdgcn_ds_read_tr16_b64_v4i16((LDSP s16x4*)((LDSP char*)atf[k & 3] + 4096 * (k >> 2)));
      s16x4 b1 = __builtin_amdgcn_ds_read_tr16_b64_v4i16((LDSP s16x4*)((LDSP char*)atf[k & 3] + 4096 * (k >> 2) + 512));
      tf[t][k] = __builtin_shufflevector(b0, b1, 0, 1, 2, 3, 4, 5, 6, 7);
    }
  };
#pragma unroll
  for (int s = 0; s < 16; ++s) ckread(0, s);
  tfread(0, 0, 16);
  SCHED_FENCE();
#pragma unroll
  for (int k = 0; k < QN; ++k) {
    const int G = 192 * (w & 1) + 64 * k + lane, hl = G / 24, rec = G % 24, u = rec / 3, j = rec - 3 * u, sb = 16 * (w >> 1) + 2 * u;
    pa64[k] = L_Q + hl * 1024 + (((sb + (j >> 1)) ^ hl) << 4) + 8 * (j & 1);
    pa32[k] = L_Q + hl * 1024 + (((sb + 1) ^ hl) << 4) + (j == 2 ? 4 : 8 + 4 * j);
    PIN(pa64[k]); PIN(pa32[k]);
  }
  u32 aq = L_Q + c16 * 1024 + ((r ^ c16) << 4);
  PIN(aq); PIN(uvo);
#pragma unroll
  for (int s = 0; s < 16; ++s) { PIN(ck[0][s]); PIN(tf[0][s]); }
  SCHED_FENCE();
  wg_barrier();
  SCHED_FENCE();
  rows(24, 24 + SP0);
#pragma unroll
  for (int n = 16; n < 32; ++n) {
    tile(n, n + 1);
    if (n + 8 + SP0 == 32) { row_issue<QN>(rs, qoff, qv); SCHED_FENCE(); }
    rows(n + 8 + SP0, n + 9 + SP0);
  }
  u32 amx = L_MX + c16 * 32, asm_ = L_SM + c16 * 48 + 4 * w, apw = L_P + (w >> 1) * 1024 + lane * 16 + (w & 1) * 8, apf = L_P + lane * 16;
  u32 asr = L_SM + (4 * r + (c16 & 3)) * 48;
  u32 gb = WS_PART + (u32)((b3 * 16 + kp) * 8 + w) * PBLK, gm = c16 < 4 ? gb + 4096 + (16 * hh + 4 * r + c16) * 16 : OOB;
  gb += hh * 2048 + r * 512 + c16 * 16;
  u32 aqx[4];
#pragma unroll
  for (int k = 0; k < 4; ++k) { aqx[k] = aq ^ (u32)(64 * k); PIN(aqx[k]); }
  PIN(amx); PIN(asm_); PIN(apw); PIN(apf); PIN(asr); PIN(gb); PIN(gm);
  SCHED_FENCE();
  row_wait<QN>(rs, qoff, qv, tag, true, lane);
  SCHED_FENCE();
#pragma unroll
  for (int k = 0; k < QN; ++k) {
    *reinterpret_cast<LDSP u32x2*>((LDSP char*)pa64[k]) = u32x2{qv[k][0], qv[k][1]};
    *reinterpret_cast<LDSP u32*>((LDSP char*)pa32[k]) = qv[k][2];
  }
  SCHED_FENCE();
#pragma unroll
  for (int n = 0; n < 4; ++n) uv[n] = ldnt(reinterpret_cast<const u16*>(reinterpret_cast<const char*>(uvp + (size_t)n * (NH * DH)) + uvo));
  SCHED_FENCE();
  wg_barrier();
  SCHED_FENCE();
  if (w == 7) { const u32 t = clk32(); if (lane == 0) st_sc1(rs, WS_FLG + (u32)idx * 128, u32x4{tag, tag, t, tag}); }
  const u32 hco = WS_FLG + (u32)((w & 3) * 2 + (c16 >> 3) + 16 * (c16 & 7) + 8 * (h >> 4)) * 128 + 8;
  u32 cp2 = 0;
  u32x2 hc;
  SCHED_FENCE();
  {
    f32x4 sc[2];
    u32x4 qa[16];
    constexpr int QA = 4;
    auto qread = [&](int s) { qa[s] = *reinterpret_cast<const LDSP u32x4*>((const LDSP char*)aqx[s & 3] + (s >> 2) * 256); ckread(1, s); };
#pragma unroll
    for (int s = 0; s < QA; ++s) qread(s);
#pragma unroll
    for (int s = 0; s < 16; ++s) {
      SCHED_FENCE();
      if (s + QA < 16) qread(s + QA);
      SCHED_FENCE();
#pragma unroll
      for (int t = 0; t < 2; ++t) sc[t] = mfma16(asbf(ck[t][s]), asbf(qa[s]), s ? sc[t] : f32x4{0.f, 0.f, 0.f, 0.f});
    }
    SCHED_FENCE();
    {
      float m = fmaxf(fmaxf(fmaxf(sc[0][0], sc[0][1]), fmaxf(sc[0][2], sc[0][3])), fmaxf(fmaxf(sc[1][0], sc[1][1]), fmaxf(sc[1][2], sc[1][3])));
      m = x32_max(x16_max(m));
      *reinterpret_cast<LDSP float*>((LDSP char*)amx + 4 * w) = m;
      SCHED_FENCE();
      wg_barrier();
      SCHED_FENCE();
      const f32x4 m0 = *reinterpret_cast<const LDSP f32x4*>((const LDSP char*)amx), m1 = *reinterpret_cast<const LDSP f32x4*>((const LDSP char*)amx + 16);
      m = fmaxf(fmaxf(fmaxf(m0[0], m0[1]), fmaxf(m0[2], m0[3])), fmaxf(fmaxf(m1[0], m1[1]), fmaxf(m1[2], m1[3]))) * SCL2;
      u32x2 p0, p1;
      p0[0] = pk2(__builtin_amdgcn_exp2f(sc[0][0] * SCL2 - m), __builtin_amdgcn_exp2f(sc[0][1] * SCL2 - m));
      p0[1] = pk2(__builtin_amdgcn_exp2f(sc[0][2] * SCL2 - m), __builtin_amdgcn_exp2f(sc[0][3] * SCL2 - m));
      p1[0] = pk2(__builtin_amdgcn_exp2f(sc[1][0] * SCL2 - m), __builtin_amdgcn_exp2f(sc[1][1] * SCL2 - m));
      p1[1] = pk2(__builtin_amdgcn_exp2f(sc[1][2] * SCL2 - m), __builtin_amdgcn_exp2f(sc[1][3] * SCL2 - m));
      *reinterpret_cast<LDSP u32x2*>((LDSP char*)apw) = p0;
      *reinterpret_cast<LDSP u32x2*>((LDSP char*)apw + 4096) = p1;
      SCHED_FENCE();
      const float ls = x32_sum(x16_sum(dot2(p0[1], 0x3f803f80u, dot2(p0[0], 0x3f803f80u, 0.f)) + dot2(p1[1], 0x3f803f80u, dot2(p1[0], 0x3f803f80u, 0.f))));
      *reinterpret_cast<LDSP float*>((LDSP char*)asm_) = ls;
      *reinterpret_cast<LDSP float*>((LDSP char*)asm_ + 32 - 4 * w) = m;
    }
    wg_barrier();
    SCHED_FENCE();
    u32x4 pf[8];
#pragma unroll
    for (int k = 0; k < 4; ++k) pf[k] = *reinterpret_cast<const LDSP u32x4*>((const LDSP char*)apf + k * 1024);
    SCHED_FENCE();
    hc = ld_sc1_64(rs, hco);
    SCHED_FENCE();
    f32x4 acc[4], l0, l1;
    u32 lw = 0, c0 = 0, mw = 0;
#pragma unroll
    for (int n = 0; n < 32; ++n) {
      acc[n & 3] = mfma16(asbf(pf[n >> 2]), tf[n >> 4][n & 15], n < 4 ? f32x4{0.f, 0.f, 0.f, 0.f} : acc[n & 3]);
      SCHED_FENCE();
      if (n < 4) pf[4 + n] = *reinterpret_cast<const LDSP u32x4*>((const LDSP char*)apf + (4 + n) * 1024);
      tfread(1, n, n + 1);
      if (n == 16) {
        l0 = *reinterpret_cast<const LDSP f32x4*>((const LDSP char*)asr); l1 = *reinterpret_cast<const LDSP f32x4*>((const LDSP char*)asr + 16);
        mw = *reinterpret_cast<const LDSP u32*>((const LDSP char*)asr + 32);
      }
      if (n == 20 && w == 6) {
        u64 pc; asm volatile("s_getpc_b64 %0" : "=s"(pc));
        cp2 = *reinterpret_cast<const __attribute__((address_space(1))) u32*>((pc & ~127ull) + min(lane, CPL2) * 128);
      }
      if (n == 24) { lw = __float_as_uint(((l0[0] + l0[1]) + (l0[2] + l0[3])) + ((l1[0] + l1[1]) + (l1[2] + l1[3]))); c0 = tag + mw + lw; PIN(lw); PIN(c0); }
      SCHED_FENCE();
    }
    SCHED_FENCE();
    u32 cs[2];
#pragma unroll
    for (int ip = 0; ip < 2; ++ip) {
      const u32 a0 = pk2(acc[0][2 * ip], acc[1][2 * ip]), a1 = pk2(acc[2][2 * ip], acc[3][2 * ip]);
      const u32 b0 = pk2(acc[0][2 * ip + 1], acc[1][2 * ip + 1]), b1 = pk2(acc[2][2 * ip + 1], acc[3][2 * ip + 1]);
      const u32x4 x = u32x4{dppb<0x128, 0xc>(a0, b0) + tag, dppb<0x128, 0xc>(a1, b1), dppb<0x128, 0x3>(b0, a0), dppb<0x128, 0x3>(b1, a1)};
      st_sc1(rs, gb + ip * 256, x);
      cs[ip] = (x[0] + x[1]) + (x[2] + x[3]);
    }
    {
      const bool o2 = c16 & 2;
      u32 c = (o2 ? cs[1] : cs[0]) + dppu<0x141>(o2 ? cs[0] : cs[1]);
      c += dppu<0xB1>(c);
      c += dppb<0x124, 0xa>(dppb<0x12c, 0x5>(0u, c), c);
      c = (c16 & 1) ? dppu<0x128>(c) : c;
      st_sc1(rs, gm, u32x4{mw, lw, c + c0, tag});
    }
    SCHED_FENCE();
  }
  bf16x8 uvf[4];
#pragma unroll
  for (int n = 0; n < 4; ++n) {
    const int c = 16 * w + 4 * r + n;
    *reinterpret_cast<u32x4*>(smem + L_T + w * 16384 + (4 * r + n) * 256 + ((c16 ^ (2 * (((c >> 4) & 3) | ((c & 1) << 2)))) << 4)) = uv[n];
  }
  wg_barrier();
  SCHED_FENCE();
  u32 lrd = L_LAT + ((c16 & 4) ? 4 + (~c16 & 3) : (c16 & 3)) * 272 + 16 * r;
  const u32 word = 512 * e4 + tid;
  u32 cxa = WS_ACX + (u32)(h * 512 + tid) * 8, exw = L_EX + (w * 128 + 8 * c16 + ((2 * r) ^ (c16 & 7))) * 16;
  u32 aoff = WS_ACC + 65536 + (word >> 3) * 256 + (word & 7) * 8, ooff;
  { const u32 col = (tid >> 2) ^ ((tid >> 5) & 7); ooff = (((col & 2) ? (~tid & 3) : (tid & 3)) * 1024 + 512 + 128 * e4 + col) * 2; }
  u32 mk[4], mq[4], m4 = (lane & 4) ? ~0u : 0u, m8 = (lane & 8) ? ~0u : 0u, mh = (lane & 4) ? 0xffff0000u : 0xffffu, lrx = L_EX + tid * 4, tag9 = 9 * tag;
#pragma unroll
  for (int k = 0; k < 4; ++k) { mk[k] = ((lane & 7) >> 1) == k ? ((lane & 1) ? 0xffff0000u : 0xffffu) : 0u; mq[k] = (c16 >> 2) == k ? ~0u : 0u; PIN(mk[k]); PIN(mq[k]); }
  PIN(m4); PIN(m8); PIN(mh); PIN(lrx); PIN(tag9);
  const FxC fc = fx_const(), fd = fx_const();
  u64 owb = (u64)owp;
  asm volatile("" : "+s"(owb));
  {
    const u32 pblk = WS_PART + (u32)(((w & 3) * 16 + (lane >> 3)) * 8 + 2 * e4 + (w >> 2)) * PBLK;
    u32 off[2], moff = pblk + (lane & 4) * (16 * PBLK) + 4096 + h * 16; u32x4 v[2], mv;
#pragma unroll
    for (int t = 0; t < 2; ++t) { off[t] = pblk + h * 128 + ((lane & 7) << 4) + (u32)(t * 64 * PBLK); PIN(off[t]); }
    PIN(moff); PIN(owo);
    if (idx == 0 && w == 7) {
      const u32 eoff = WS_QX + (u32)((lane >> 2) * 16 + (lane & 3)) * DH * 4;
#pragma unroll 1
      for (u32 spins = 0; spins < SPIN_CAP; ++spins) {
        const u32x4 e0 = ld_sc1(rs, eoff), e1 = ld_sc1(rs, eoff + 16), e2 = ld_sc1(rs, eoff + 16 * 16 * DH * 4), e3 = ld_sc1(rs, eoff + 16 * 16 * DH * 4 + 16), e4_ = ld_sc1(rs, WS_FLG + 8 * 128);
        if (__all((qtag(e0) == tag) & (qtag(e1) == tag) & (qtag(e2) == tag) & (qtag(e3) == tag) & (e4_[0] == tag) & (e4_[1] == tag))) break;
        asm volatile("" ::: "memory");
      }
      if (lane == 0) st_sc1_32(rs, WS_EPOCH, tag);
    }
    hint_wait(rs, hco, hc, tag, THC);
    asm volatile("" ::: "memory");
    mv = ld_sc1(rs, moff);
    SCHED_FENCE();
    row_issue<2>(rs, off, v);
    SCHED_FENCE();
#pragma unroll
    for (int s = 0; s < 4; ++s) {
      const u32 tq = c16 >> 2, tp = lane & 3, sw = 2 * (tq | (4 * (r & 1)));
      const char* p = smem + L_T + (4 * (s >> 1) + tq) * 16384 + (4 * (s & 1) + r) * 256 + (((2 * w + (tp >> 1)) ^ sw) << 4) + 8 * (tp & 1);
      s16x4 b0 = __builtin_amdgcn_ds_read_tr16_b64_v4i16((LDSP s16x4*)p);
      s16x4 b1 = __builtin_amdgcn_ds_read_tr16_b64_v4i16((LDSP s16x4*)(p + 8 * 256));
      uvf[s] = __builtin_shufflevector(b0, b1, 0, 1, 2, 3, 4, 5, 6, 7);
      PIN(uvf[s]);
    }
    u32 lad = (c16 >> 3) == (r >> 1) ? L_LAT + (4 * (r >> 1) + (w & 3)) * 272 + (w >> 2) * 128 + (c16 & 7) * 16 + (r & 1) * 8 : L_LAT + 4096 + tid * 8;
    PIN(lrd);
    PIN(lad); PIN(ooff); PIN(aoff); PIN(cxa); PIN(exw);
    SCHED_FENCE();
    auto comb = [&](bool first) {
      float M, T;
      asm volatile("v_max_f32_dpp %0, %2, %2 row_half_mirror row_mask:0xf bank_mask:0xf\n\ts_nop 1\n\tv_max_f32_dpp %0, %0, %0 row_ror:8 row_mask:0xf bank_mask:0xf\n\t"
                   "v_mov_b32 %1, %0\n\ts_nop 1\n\tv_permlane16_swap_b32 %0, %1\n\tv_max_f32 %0, %0, %1\n\t"
                   "v_mov_b32 %1, %0\n\ts_nop 1\n\tv_permlane32_swap_b32 %0, %1\n\tv_max_f32 %0, %0, %1" : "=&v"(M), "=&v"(T) : "v"(mv[0]));
      const float ex = __builtin_amdgcn_exp2f(__uint_as_float(mv[0]) - M);
      const u32 eb = pk2(ex, ex), a0 = eb & mk[0], a1 = eb & mk[1];
      SCHED_FENCE();
      u32 eo = dppu<0x141>(eb);
      PIN(eo);
      SCHED_FENCE();
      const u32 b2 = eb & mk[2], b3 = eb & mk[3];
      SCHED_FENCE();
      const f32x4 d0 = mfma16(asbf(u32x4{a0, a1, eo & mk[2], eo & mk[3]}), asbf(v[0]), f32x4{0.f, 0.f, 0.f, 0.f});
      const f32x4 d = mfma16(asbf(u32x4{eo & mk[0], eo & mk[1], b2, b3}), asbf(v[1]), d0);
      SCHED_FENCE();
      if (first) {
#pragma unroll
        for (int n = 0; n < 4; ++n) ow[n] = __builtin_nontemporal_load(reinterpret_cast<const __attribute__((address_space(1))) u32x4*>(owb + n * (DM * 2) + owo));
      }
      SCHED_FENCE();
      u32 rl, s0, s1, t0, t1;
      asm volatile("v_and_b32 %0, 0xffff0000, %5\n\tv_add_u32 %1, %10, %11\n\tv_mul_f32 %0, %0, %7\n\tv_add_u32 %2, %14, %15\n\tv_add_u32 %3, %12, %13\n\t"
                   "v_add_f32_dpp %0, %0, %0 row_half_mirror row_mask:0xf bank_mask:0xf\n\tv_add_u32 %4, %16, %17\n\tv_add_u32 %1, %1, %3\n\t"
                   "v_add_f32_dpp %0, %0, %0 row_ror:8 row_mask:0xf bank_mask:0xf\n\tv_add_u32 %2, %2, %4\n\tv_mov_b32 %3, %0\n\t"
                   "v_bitop3_b32 %4, %18, %2, %1 bitop3:0xca\n\tv_bitop3_b32 %1, %18, %1, %2 bitop3:0xca\n\t"
                   "v_permlane16_swap_b32 %0, %3\n\tv_add_f32 %0, %0, %3\n\t"
                   "v_add_u32_dpp %1, %1, %4 row_half_mirror row_mask:0xf bank_mask:0xf\n\tv_mov_b32 %3, %0\n\tv_add_u32 %2, %6, %7\n\t"
                   "v_add_u32_dpp %1, %1, %1 quad_perm:[1,0,3,2] row_mask:0xf bank_mask:0xf\n\t"
                   "v_permlane32_swap_b32 %0, %3\n\tv_add_f32 %0, %0, %3\n\t"
                   "v_add_u32_dpp %1, %1, %1 quad_perm:[2,3,0,1] row_mask:0xf bank_mask:0xf\n\t"
                   "v_rcp_f32 %0, %0\n\tv_add_u32 %2, %2, %20\n\tv_sub_u32 %2, %2, %8\n\tv_add_u32 %1, %1, %2\n\t"
                   "v_mul_f32 %2, %21, %0\n\tv_mul_f32 %3, %22, %0\n\tv_mul_f32 %4, %23, %0\n\tv_mul_f32 %0, %24, %0\n\t"
                   "v_cvt_pk_bf16_f32 %2, %2, %3\n\tv_cvt_pk_bf16_f32 %3, %4, %0"
                   : "=&v"(rl), "=&v"(s0), "=&v"(s1), "=&v"(t0), "=&v"(t1)
                   : "v"(eb), "v"(mv[0]), "v"(mv[1]), "v"(mv[2]), "v"(mv[3]), "v"(v[0][0]), "v"(v[0][1]), "v"(v[0][2]), "v"(v[0][3]),
                     "v"(v[1][0]), "v"(v[1][1]), "v"(v[1][2]), "v"(v[1][3]), "v"(m4), "v"(d0), "v"(tag9), "v"(d[0]), "v"(d[1]), "v"(d[2]), "v"(d[3]));
      *reinterpret_cast<LDSP u32x2*>((LDSP char*)lad) = u32x2{s1, t0};
      SCHED_FENCE();
      return __ballot(__builtin_amdgcn_bitop3_b32(s0, mv[3], tag, 0xf6) != 0u);
    };
    asm volatile("" :: "v"(cp2));
#pragma unroll
    for (int t = 0; t < 2; ++t) v[t][0] -= tag;
    u64 bm = comb(true);
    if (__builtin_expect(bm != 0ull, 0)) {
#pragma unroll 1
      for (u32 spins = 0; spins < SPIN_CAP; ++spins) {
        __builtin_amdgcn_s_sleep(2);
        asm volatile("" ::: "memory");
        const u32 q = (u32)(bm >> (lane & 56));
        u32x4 t4[2];
#pragma unroll
        for (int t = 0; t < 2; ++t) t4[t] = ld_sc1(rs, ((q >> (4 * t)) & 15) ? off[t] : OOB);
        const bool rm = (q >> (lane & 4)) & 15;
        const u32x4 t2 = ld_sc1(rs, rm ? moff : OOB);
#pragma unroll
        for (int t = 0; t < 2; ++t) { t4[t][0] -= tag; v[t] = ((q >> (4 * t)) & 15) ? t4[t] : v[t]; }
        mv = rm ? t2 : mv;
        PIN(mv);
        asm volatile("s_nop 1");
        bm = comb(false);
        if (bm == 0ull) break;
      }
    }
    SCHED_FENCE();
  }
  wg_barrier();
  SCHED_FENCE();
  {
    bf16x8 lf[4];
#pragma unroll
    for (int s = 0; s < 4; ++s) lf[s] = *reinterpret_cast<const LDSP bf16x8*>((const LDSP char*)lrd + 64 * s);
    SCHED_FENCE();
    f32x4 c = f32x4{0.f, 0.f, 0.f, 0.f};
#pragma unroll
    for (int s = 0; s < 4; ++s) c = mfma16(uvf[s], lf[s], c);
    const float ya = self(m8, c[2], c[0]), yb = self(m8, c[3], c[1]);
    const float mine = self(m4, yb, ya) + dppf<0x141>(self(m4, ya, yb));
    u64 t = fx_add2<4>(a.ws, rs, mine, cxa, WS_XC + (u32)((h * 4 + e4) * 512 + tid) * 16, tag, fc);
    u32x4 owf[4];
#pragma unroll
    for (int k = 0; k < 4; ++k) {
      const u32x2 f0 = __builtin_bit_cast(u32x2, kfrag(ow, 4 * (k >> 1) + (k & 1))), f1 = __builtin_bit_cast(u32x2, kfrag(ow, 4 * (k >> 1) + (k & 1) + 2));
      owf[k] = u32x4{f0[0], f0[1], f1[0], f1[1]}; PIN(owf[k]);
    }
    SCHED_FENCE();
    float x = fx_dec(t, fc.dn);
    if (__builtin_expect(!__all(__builtin_fabsf(x) < fc.rg), 0)) {
#pragma unroll 1
      for (u32 spins = 0; spins < SPIN_CAP; ++spins) {
        if (__all((u32)(fx_raw<4>(t) >> 58) == 4u)) { x = fx_val<4, 8192>(rs, fx_raw<4>(t), WS_XC + (u32)(h * 2048 + tid) * 16, tag); break; }
        u64 z = 0; PIN(z);
        t = __hip_atomic_fetch_add(reinterpret_cast<u64*>(a.ws + cxa), z, __ATOMIC_RELAXED, __HIP_MEMORY_SCOPE_AGENT) + ((0x433ull << 52) - (4ull << 58));
        x = fx_dec(t, -0x1.4p12);
        if (__all(__builtin_fabsf(x) < fc.rg)) break;
      }
    }
    const u32 xb = pk2(x, x) & mh;
    const u32 P = xb | dppu<0x141>(xb);
    const u32 p0 = P & mq[0], p1 = P & mq[1];
    SCHED_FENCE();
    u32 Q = dppu<0x128>(P);
    PIN(Q);
    const u32x4 a0 = u32x4{p0, Q & mq[0], p1, Q & mq[1]}, a1 = u32x4{Q & mq[2], P & mq[2], Q & mq[3], P & mq[3]};
    f32x4 s0 = mfma16(asbf(a0), asbf(owf[0]), f32x4{0.f, 0.f, 0.f, 0.f}), s1 = mfma16(asbf(a0), asbf(owf[1]), f32x4{0.f, 0.f, 0.f, 0.f});
    s0 = mfma16(asbf(a1), asbf(owf[2]), s0); s1 = mfma16(asbf(a1), asbf(owf[3]), s1);
    *reinterpret_cast<LDSP f32x4*>((LDSP char*)exw) = s0;
    *reinterpret_cast<LDSP f32x4*>((LDSP char*)(exw ^ 16u)) = s1;
  }
  wg_barrier();
  {
    float p[8];
#pragma unroll
    for (int k = 0; k < 8; ++k) p[k] = *reinterpret_cast<const LDSP float*>((const LDSP char*)lrx + 2048 * k);
#pragma unroll
    for (int k = 1; k < 8; ++k) p[0] += p[k];
    const u64 t = fx_add2<32>(a.ws, rs, p[0], aoff, WS_XB + h * 32768 + word * 16, tag, fd);
    fx_out<32>(reinterpret_cast<const char*>(a.out), rs, t, ooff, WS_XB + word * 16, tag, fd);
  }
}

DI void roleA(const KArgs& a, char* smem, const int g, const int idx, const int w, const int lane, const u32x4 (&R)[4], const u32 kvo, const u32 cpf) {
  const int r = lane >> 4, c16 = lane & 15, tid = w * 64 + lane;
  const int h = 4 * (idx & 7) + ((idx >> 3) & 3), b = idx >> 5;
  const __amdgpu_buffer_rsrc_t rs = __builtin_amdgcn_make_buffer_rsrc((void*)a.ws, 0, (int)WS_END, RSRC_W3);
  u32 tag = *reinterpret_cast<const u32*>(a.ws + WS_EPOCH);
  u32x4 kv[16][4];
  u32x4 ov[16];
  const u16* wop = a.woa + (size_t)(h * DH + 64 * (c16 >> 3) + 8 * r) * DM + 64 * w + 8 * (c16 & 7);
  auto wq4 = [&](int q) { return qoffs(q, b); };
  u64 wlate[2] = {(u64)(wop + (size_t)wq4(2) * DM), (u64)(wop + (size_t)wq4(3) * DM)};
  auto woload = [&](int n) {
    if (n == 7) { PIN(wlate[0]); PIN(wlate[1]); }
    if (n < 8) ov[n] = ldsh(wop + (size_t)(wq4(n >> 2) + (n & 3)) * DM);
    else ov[n] = *reinterpret_cast<const __attribute__((address_space(1))) u32x4*>(wlate[(n >> 2) & 1] + (n & 3) * (DM * 2));
  };
  const u32 org = clk32();
  const int rot = (w >> 2) * FW2;
  auto kvissue = [&](int lo, int hi) {
#pragma unroll
    for (int n = 0; n < 64; ++n) {
      if (n < lo || n >= hi) continue;
      const int ch = n >> 2, isv = ch < C0 ? ch & 1 : ch >= C0 + ND, st = ch < C0 ? ch >> 1 : 8 - ND + (ch - C0) % ND;
      if (n >= NA0 && n < NA1) soft<WSA>(org, TA0 + n * TA1);
      asm volatile("" ::: "memory");
      kv[ch][n & 3] = ldnt(reinterpret_cast<const u16*>(reinterpret_cast<const char*>(isv ? a.vc : a.kc) + (16 * (st ^ rot) + 4 * (n & 3)) * (DH * 2) + kvo));
      SCHED_FENCE();
    }
  };
  auto upto = [](int ch) { const int y = PY + CU * (ch + 1), x = 4 * (ch + AW + 1), z = ch < 0 ? PY : (x > y ? x : y); return z < 4 * NCH ? z : 4 * NCH; };
  {
    const int m0 = 128 * b + 16 * w + 4 * r;
    const u32x2 xf = ldnt8(a.xa + (c16 & 3) * DM + m0);
    SCHED_FENCE();
    __builtin_amdgcn_s_barrier();
    SCHED_FENCE();
    kvissue(0, A0);
    SCHED_FENCE();
    asm volatile("" :: "v"(cpf));
    PIN(tag); tag = next_tag(tag);
    f32x4 ac[8];
#pragma unroll
    for (int j = 0; j < 8; ++j) ac[j] = __builtin_amdgcn_mfma_f32_16x16x16bf16_1k(__builtin_bit_cast(s16x4, xf), kfrag(R, j), f32x4{0.f, 0.f, 0.f, 0.f}, 0, 0, 0);
    const f32x4 s0 = sel4(ac[0], ac[2], ac[4], ac[6], r), s1 = sel4(ac[1], ac[3], ac[5], ac[7], r);
    float* pp = reinterpret_cast<float*>(smem + L_AX) + w * 512 + 8 * c16 + 2 * r;
#pragma unroll
    for (int k = 0; k < NB; ++k) *reinterpret_cast<u32x2*>(pp + k * DH) = u32x2{__float_as_uint(s0[k]), __float_as_uint(s1[k])};
  }
  wg_barrier();
  if (w < 2) {
    const float* pp = reinterpret_cast<const float*>(smem + L_AX) + 4 * (tid & 127);
    f32x4 t[8];
#pragma unroll
    for (int k = 0; k < 8; ++k) t[k] = *reinterpret_cast<const f32x4*>(pp + 512 * k);
    f32x4 s = ((t[0] + t[1]) + (t[2] + t[3])) + ((t[4] + t[5]) + (t[6] + t[7]));
    PIN(s);
    u32x4 x;
#pragma unroll
    for (int k = 0; k < 4; ++k) x[k] = __builtin_amdgcn_perm(__float_as_uint(s[k]), tag, 0x07060500u + k);
    st_sc1(rs, WS_QX + (u32)(((h * 4 + ((tid >> 5) & 3)) * 4 + b) * DH + 4 * (lane & 31)) * 4, x);
  }
  SCHED_FENCE();
  __builtin_amdgcn_s_barrier();
  SCHED_FENCE();
  kvissue(A0, PX);
  SCHED_FENCE();
  u32x4 qpk;
  {
    const u32 off = WS_QX + (u32)(((h * 4 + b) * 4 + r) * DH + 64 * (w & 1) + 4 * c16) * 4;
    u32x4 v = u32x4{0u, 0u, 0u, 0u};
    if ((w >> 1) == 1) v = ld_sc1(rs, off);
    SCHED_FENCE();
    kvissue(PX, PY);
    SCHED_FENCE();
    if ((w >> 1) == 1) q_finish(rs, off, v, tag, smem + L_AQ + 128 * (w & 1), lane);
    wg_barrier();
    qpk = *reinterpret_cast<const u32x4*>(smem + L_AQ + 16 * c16);
  }
  SCHED_FENCE();
  float m = -INFINITY, l = 0.f, sd[ND];
  f32x4 acc = f32x4{0.f, 0.f, 0.f, 0.f};
  u32 pm[4], pb[ND];
  u32x4 pa[4];
#pragma unroll
  for (int k = 0; k < 4; ++k) { pm[k] = (c16 >> 1) == k ? ((c16 & 1) ? 0xffff0000u : 0xffffu) : 0u; PIN(pm[k]); }
  auto pbuild = [&](u32 x) {
    u32 t[4] = {dppu<0x150>(x), dppu<0x154>(x), dppu<0x158>(x), dppu<0x15c>(x)};
#pragma unroll
    for (int n = 0; n < 4; ++n) { PIN(t[n]); pa[n] = u32x4{t[n] & pm[0], t[n] & pm[1], t[n] & pm[2], t[n] & pm[3]}; PIN(pa[n]); }
  };
  u32x4 wf[2][8];
  auto wpack = [&](int q) {
#pragma unroll
    for (int j = 0; j < 8; ++j) {
      const u32x2 t = __builtin_bit_cast(u32x2, kfrag(ov + 4 * q, j));
      wf[q >> 1][j][2 * (q & 1)] = t[0]; wf[q >> 1][j][2 * (q & 1) + 1] = t[1];
      PIN(wf[q >> 1][j]);
    }
  };
  const bool hi8 = c16 & 8, hi4 = c16 & 4;
  u32 aaf = L_AST + (c16 >> 1) * 288 + (64 * (c16 & 1) + 8 * r) * 2, aml = L_AST + 2304 + 8 * (lane & 7), m8 = hi8 ? ~0u : 0u;
  u32 asw = r < 2 ? L_AST + w * 288 + (64 * (c16 >> 3) + 8 * (c16 & 3) + qoffs(((c16 >> 1) & 2) + r, 4 - b)) * 2 : L_AX + tid * 8;
  PIN(aaf); PIN(m8);
  const FxC fc = fx_const();
  u32 aoff = WS_ACC + b * 16384 + (tid >> 3) * 256 + (tid & 7) * 8, ooff = (b * 1024 + 64 * w + 8 * (c16 & 7) + 2 * r + (c16 >> 3)) * 2;
  PIN(aml); PIN(asw); PIN(aoff); PIN(ooff);
  const u32 foff = WS_FLG + (u32)(idx ^ (lane & 8)) * 128;
  u32x2 fl = u32x2{tag, tag};
  auto wit = [](int n) { return n < 8 ? n : n < 12 ? WL1 : WL2; };
  auto wdn = [&](int n) { return upto(n) / 4; };
  auto steps = [&](const int lo, const int hi) __attribute__((always_inline)) {
#pragma unroll
  for (int ch = 0; ch < NCH; ++ch) {
    if (ch < lo || ch >= hi) continue;
    SCHED_FENCE();
    kvissue(upto(ch - 1), upto(ch));
#pragma unroll
    for (int n = 0; n < 16; ++n)
      if (wit(n) == ch) woload(n);
    SCHED_FENCE();
    if (ch == FLI && w == FLW) fl = __builtin_amdgcn_raw_buffer_load_b64(rs, foff, 0, 16);
    SCHED_FENCE();
    if (ASYNC > 0 && ch % ASYNC == ASYNC - 1 && ch < C0 + ND - 1) { __builtin_amdgcn_s_barrier(); SCHED_FENCE(); }
    if (ch < C0 && !(ch & 1)) {
      const float s = score4(kv[ch], qpk, hi8, hi4);
      if (__builtin_expect(__any(s > m), ch < 2)) {
        float mn = fmaxf(fmaxf(s, dppf<0x128>(s)), m);
        mn = x32_max(x16_max(fmaxf(mn, dppf<0x124>(mn))));
        const float alpha = __builtin_amdgcn_exp2f(m - mn);
        m = mn;
        l *= alpha;
        acc *= alpha;
      }
      const float p = __builtin_amdgcn_exp2f(s - m);
      l += p;
      pbuild(pk2(p, p));
      PIN(l);
    } else if (ch >= C0 && ch < C0 + ND - 1) {
      sd[ch - C0] = score4(kv[ch], qpk, hi8, hi4);
      PIN(sd[ch - C0]);
    } else if (ch == C0 + ND - 1) {
      sd[ND - 1] = score4(kv[ch], qpk, hi8, hi4);
      float mn = sd[0];
#pragma unroll
      for (int k = 1; k < ND; ++k) mn = fmaxf(mn, sd[k]);
      mn = fmaxf(fmaxf(mn, dppf<0x128>(mn)), m);
      mn = x32_max(x16_max(fmaxf(mn, dppf<0x124>(mn))));
      const float beta = __builtin_amdgcn_exp2f(m - mn);
      l *= beta;
#pragma unroll
      for (int k = 0; k < ND; ++k) { sd[k] = __builtin_amdgcn_exp2f(sd[k] - mn); l += sd[k]; }
      l += dppf<0x128>(l); l += dppf<0x124>(l);
      l = x32_sum(x16_sum(l));
      if (lane == 0) *reinterpret_cast<f32x2_*>(smem + L_AST + 2304 + 8 * w) = f32x2_{mn, l};
      wg_barrier();
      const f32x2_ ml = *reinterpret_cast<const LDSP f32x2_*>((const LDSP char*)aml);
      float M = fmaxf(ml[0], dppf<0xB1>(ml[0]));
      M = fmaxf(M, dppf<0x4E>(M)); M = fmaxf(M, dppf<0x141>(M));
      float L = __builtin_amdgcn_exp2f(ml[0] - M) * ml[1];
      L += dppf<0xB1>(L); L += dppf<0x4E>(L); L += dppf<0x141>(L);
      const float al = __builtin_amdgcn_exp2f(mn - M) * __builtin_amdgcn_rcpf(L), ab = al * beta;
      acc *= ab; PIN(acc);
#pragma unroll
      for (int k = 0; k < ND; ++k) { pb[k] = pk2(sd[k] * al, sd[k] * al); PIN(pb[k]); }
      pbuild(pb[0]);
    } else {
#pragma unroll
      for (int n = 0; n < 4; ++n) acc = mfma16(asbf(pa[n]), asbf(kv[ch][n]), acc);
      PIN(acc);
      SCHED_FENCE();
      if (ch >= C0 + ND && ch < NCH - 1) pbuild(pb[ch + 1 - C0 - ND]);
    }
#pragma unroll
    for (int q = 0; q < 2; ++q)
      if (wdn(4 * q + 3) - 1 == ch) wpack(q);
  }
  };
  steps(0, NCH - 1);
  if (__builtin_expect(!__all((fl[0] == tag) & (fl[1] == tag)), 0)) {
#pragma unroll 1
    for (u32 spins = 0; spins < SPIN_CAP; ++spins) {
      asm volatile("" ::: "memory");
      fl = __builtin_amdgcn_raw_buffer_load_b64(rs, foff, 0, 16);
      if (__all((fl[0] == tag) & (fl[1] == tag))) break;
      __builtin_amdgcn_s_sleep(2);
    }
  }
  steps(NCH - 1, NCH);
#pragma unroll
  for (int n = 0; n < 16; ++n)
    if (wit(n) == NCH) woload(n);
  SCHED_FENCE();
  *reinterpret_cast<LDSP u32x2*>((LDSP char*)asw) = u32x2{pk2(acc[0], acc[1]), pk2(acc[2], acc[3])};
  SCHED_FENCE();
  wg_barrier();
  {
    const bf16x8 af0 = *reinterpret_cast<const LDSP bf16x8*>((const LDSP char*)aaf), af1 = *reinterpret_cast<const LDSP bf16x8*>((const LDSP char*)aaf + 64);
    SCHED_FENCE();
    f32x4 D[8];
    float u[4], v[2];
    auto JO = [](int j) { return (j & 1) | ((j & 2) << 1) | ((j & 4) >> 1); };
    auto last = [&](int j) { const u32x2 t = __builtin_bit_cast(u32x2, kfrag(ov + 12, j)); wf[1][j][2] = t[0]; wf[1][j][3] = t[1]; PIN(wf[1][j]); };
    auto pair = [&](int k) {
      const f32x4 e = D[2 * k], o = D[2 * k + 1];
      const float sn = self(m8, e[1], o[0]) + self(m8, e[3], o[2]);
      SCHED_FENCE();
      u[k] = (self(m8, o[1], e[0]) + self(m8, o[3], e[2])) + dppf<0x128>(sn);
      SCHED_FENCE();
    };
    auto quad = [&](int k) { auto x = __builtin_amdgcn_permlane32_swap(__float_as_uint(u[k]), __float_as_uint(u[k + 2]), false, false); v[k] = __uint_as_float(x[0]) + __uint_as_float(x[1]); SCHED_FENCE(); };
#pragma unroll
    for (int j = 0; j < 8; ++j) {
      D[JO(j)] = mfma16(af0, asbf(wf[0][JO(j)]), f32x4{0.f, 0.f, 0.f, 0.f});
      SCHED_FENCE();
      wf[1][JO(j)] = __builtin_bit_cast(u32x2, kfrag(ov + 8, JO(j))).xyxy;
      PIN(wf[1][JO(j)][0]); PIN(wf[1][JO(j)][1]);
      SCHED_FENCE();
    }
    last(JO(0));
#pragma unroll
    for (int j = 0; j < 8; ++j) {
      if (j < 7) last(JO(j + 1));
      SCHED_FENCE();
      D[JO(j)] = mfma16(af1, asbf(wf[1][JO(j)]), D[JO(j)]);
      SCHED_FENCE();
      if (j == 4) pair(0);
      if (j == 6) pair(2);
      if (j == 7) {
        const float sn = self(m8, D[2][1], D[3][0]) + self(m8, D[2][3], D[3][2]);
        SCHED_FENCE();
        const float kp = self(m8, D[3][1], D[2][0]) + self(m8, D[3][3], D[2][2]);
        SCHED_FENCE();
        quad(0);
        u[1] = kp + dppf<0x128>(sn);
        SCHED_FENCE();
        const float k1 = self(m8, D[7][1], D[6][0]);
        SCHED_FENCE();
        const float s3 = self(m8, D[6][1], D[7][0]) + self(m8, D[6][3], D[7][2]);
        SCHED_FENCE();
        const float k2 = self(m8, D[7][3], D[6][2]);
        SCHED_FENCE();
        u[3] = (k1 + dppf<0x128>(s3)) + k2;
        SCHED_FENCE();
        quad(1);
      }
      SCHED_FENCE();
    }
    const auto x = __builtin_amdgcn_permlane16_swap(__float_as_uint(v[0]), __float_as_uint(v[1]), false, false);
    const float yy = __uint_as_float(x[0]) + __uint_as_float(x[1]);
    u64 t = fx_add2<32>(a.ws, rs, yy, aoff, WS_XA + h * 32768 + (b * 512 + tid) * 16, tag, fc);
    fx_out<32>(reinterpret_cast<const char*>(a.out), rs, t, ooff, WS_XA + (b * 512 + tid) * 16, tag, fc);
  }
}

__global__ void __launch_bounds__(512, 2) fused(KArgs a) {
  extern __shared__ __attribute__((aligned(16))) char smem[];
  const int tid = threadIdx.x, lane = tid & 63, w = __builtin_amdgcn_readfirstlane(tid >> 6), g = blockIdx.x;
  const int idx = (g & 7) | ((g >> 4) << 3);
  u32 cpf = 0;
  if (w >= 8 - CPF && (idx >> 3) < CPN) {
    u64 pc; asm volatile("s_getpc_b64 %0" : "=s"(pc));
    cpf = *reinterpret_cast<const __attribute__((address_space(1))) u32*>((pc & ~127ull) + min((7 - w) * 64 + lane, CPL) * 128);
  }
  SCHED_FENCE();
  u32 o[4], o2;
  {
    const u32 h = 4 * (idx & 7) + ((idx >> 3) & 3), b = idx >> 5, r = lane >> 4;
#pragma unroll
    for (int n = 0; n < 4; ++n) { o[n] = (128 * b + 16 * w + 4 * r + n) * (NH * DH * 2) + h * (DH * 2) + (lane & 15) * 16; PIN(o[n]); }
    u32 ob = ((((idx & 7) * 16 + (idx >> 3)) * 128 + w) * DC + 8 * lane) * 2, oa = (((b * NH + h) * SK + (SK - WIN) + 128 * w + r) * DH + 8 * (lane & 15)) * 2;
    PIN(oa); PIN(ob);
    o2 = ((g >> 3) & 1) ? ob : oa;
    PIN(o2);
  }
  SCHED_FENCE();
  asm volatile("" :: "s"(a.xa), "s"(a.kc), "s"(a.vc), "s"(a.wqa), "s"(a.woa), "s"(a.xb), "s"(a.ckv), "s"(a.wqb), "s"(a.wuk), "s"(a.wuv), "s"(a.wob), "s"(a.ws), "s"(a.out));
  u32x4 R[4];
  {
    const char* p = reinterpret_cast<const char*>(((g >> 3) & 1) ? a.wuk : a.wqa);
#pragma unroll
    for (int n = 0; n < 4; ++n) R[n] = ldnt(reinterpret_cast<const u16*>(p + o[n]));
    SCHED_FENCE();
  }
  int g2 = g, l2 = lane;
  if (__builtin_expect((g >> 3) & 1, 0)) {
    roleB(a, smem, g, idx, w, lane, R, o2, cpf);
    asm volatile("" : "=v"(R[0]), "=v"(R[1]), "=v"(R[2]), "=v"(R[3]), "=v"(o2), "=v"(cpf), "=v"(l2));
  }
  asm volatile("" : "+s"(g2));
  if (__builtin_expect(!((g2 >> 3) & 1), 1)) roleA(a, smem, g, idx, w, l2 & 63, R, o2, cpf);
}

extern "C" void kernel_launch(void* const* d_in, const int* in_sizes, int n_in,
                              void* d_out, int out_size, void* d_ws, size_t ws_size,
                              hipStream_t stream) {
  KArgs a;
  a.xa  = (const u16*)d_in[0];
  a.kc  = (const u16*)d_in[1];
  a.vc  = (const u16*)d_in[2];
  a.wqa = (const u16*)d_in[3];
  a.woa = (const u16*)d_in[4];
  a.xb  = (const u16*)d_in[5];
  a.ckv = (const u16*)d_in[6];
  a.wqb = (const u16*)d_in[7];
  a.wuk = (const u16*)d_in[8];
  a.wuv = (const u16*)d_in[9];
  a.wob = (const u16*)d_in[10];
  {
    const size_t need = (size_t)WS_END + (1u << 20);
    const size_t off = ws_size > need ? (((ws_size - need) / 8 * WOFF) & ~(size_t)0x1fffff) : 0;
    a.ws = (char*)d_ws + off;
  }
  a.out = (u16*)d_out;
  (void)hipFuncSetAttribute((const void*)fused, hipFuncAttributeMaxDynamicSharedMemorySize, L_END);
  fused<<<256, 512, L_END, stream>>>(a);
}
```
